```python
import torch

def _softmax(x, axis=-1):
    return torch.softmax(x, dim=axis)
def _gelu(x):
    return torch.nn.functional.gelu(x, approximate="tanh")
def _silu(x):
    return torch.nn.functional.silu(x)
def _sigmoid(x):
    return torch.sigmoid(x)
def _layernorm(x, axis=-1, eps=1e-5):
    m = x.mean(dim=axis, keepdim=True)
    v = x.var(dim=axis, keepdim=True, unbiased=False)
    return (x - m) / torch.sqrt(v + eps)
def _scatter_add(tgt, idx, src, axis=0):
    return tgt.clone().index_add_(axis, idx, src)
def _segment_sum(src, seg, n):
    out = torch.zeros((n,) + src.shape[1:], dtype=src.dtype)
    return out.index_add_(0, seg, src)
def _pad(x, pw):
    flat = tuple(v for pair in reversed(pw) for v in pair)
    return torch.nn.functional.pad(x, flat)

kernel_name = 'gated_mix__ssm_conv_h3__glu_gated_geglu__prompt'

def setup_inputs():

    _w = (lambda a: (lambda t: t.to(torch.bfloat16) if t.is_floating_point() else t)(torch.as_tensor(a)))
    def _A_setup():
        B, S, D = 1, 2048, 512
        ks = 4
        return {
            "q": _w((lambda *s: torch.randn(*s, dtype=torch.bfloat16) * 0.05)(B, S, D)),
            "k": _w((lambda *s: torch.randn(*s, dtype=torch.bfloat16) * 0.05)(B, S, D)),
            "v": _w((lambda *s: torch.randn(*s, dtype=torch.bfloat16) * 0.05)(B, S, D)),
            "shift_k": _w((lambda *s: torch.randn(*s, dtype=torch.bfloat16) * 0.05)(D, ks) * 0.5),
            "ssm_k": _w((lambda *s: torch.randn(*s, dtype=torch.bfloat16) * 0.05)(D, S) * (1.0 / S ** 0.5)),
            "ssm_D": _w((lambda *s: torch.randn(*s, dtype=torch.bfloat16) * 0.05)(D)),
        }
    _A_in = _A_setup()

    def _B_setup():
        B, S, Dm, Df = 1, 2048, 512, 1792
        return {
            "x": _w((lambda *s: torch.randn(*s, dtype=torch.bfloat16) * 0.05)(B, S, Dm)),
            "w_gate": _w((lambda *s: torch.randn(*s, dtype=torch.bfloat16) * 0.1)(Dm, Df) * (Dm ** -0.5)),
            "w_up": _w((lambda *s: torch.randn(*s, dtype=torch.bfloat16) * 0.1)(Dm, Df) * (Dm ** -0.5)),
            "w_down": _w((lambda *s: torch.randn(*s, dtype=torch.bfloat16) * 0.1)(Df, Dm) * (Df ** -0.5)),
        }
    _B_in = _B_setup()

    return {
        "A_q": _A_in["q"],
        "A_k": _A_in["k"],
        "A_v": _A_in["v"],
        "A_shift_k": _A_in["shift_k"],
        "A_ssm_k": _A_in["ssm_k"],
        "A_ssm_D": _A_in["ssm_D"],
        "B_x": _B_in["x"],
        "B_w_gate": _B_in["w_gate"],
        "B_w_up": _B_in["w_up"],
        "B_w_down": _B_in["w_down"],
        "gate_w": _w((lambda *s: torch.randn(*s, dtype=torch.bfloat16) * 0.05)(512) * 16.0),
    }

def reference(A_q, A_k, A_v, A_shift_k, A_ssm_k, A_ssm_D, B_x, B_w_gate, B_w_up, B_w_down, gate_w):
    def _A_ref(q, k, v, shift_k, ssm_k, ssm_D):
        B, S, D = q.shape
        ks = shift_k.shape[-1]
        k_pad = _pad(k, ((0, 0), (ks - 1, 0), (0, 0)))
        k_shift = torch.zeros_like(k)
        for t in range(ks):
            k_shift = k_shift + k_pad[:, t:t + S, :] * shift_k[:, ks - 1 - t][None, None, :]
        kv = k_shift * v
        L = 2 * S
        kv_pad = _pad(kv, ((0, 0), (0, S), (0, 0)))
        kf_pad = _pad(ssm_k, ((0, 0), (0, S)))
        kv_f = (lambda x, axes=(-2,-1): torch.fft.rfft2(x.float(), dim=axes))(kv_pad[:, None, :, :], axes=(1, 2))
        kf_f = (lambda x, axes=(-2,-1): torch.fft.rfft2(x.float(), dim=axes))(kf_pad[:, None, :], axes=(1, 2))
        kf_f = (lambda x, *d: x.permute(*d))(kf_f, 1, 2, 0)
        y_f = kv_f * kf_f[None, :, :, :]
        y = (lambda x, s, axes=(-2,-1): torch.fft.irfft2(x, s=s, dim=axes).to(torch.bfloat16))(y_f, (1, L), axes=(1, 2))[:, 0, :S, :]
        y = y + kv * ssm_D[None, None, :]
        return q * y

    def _B_ref(x, w_gate, w_up, w_down):
        g = torch.einsum("bsd,df->bsf", x, w_gate)
        u = torch.einsum("bsd,df->bsf", x, w_up)
        h = _gelu(g) * u
        return torch.einsum("bsf,fd->bsd", h, w_down)

    x = A_q
    g = _sigmoid((x * gate_w).sum(-1, keepdims=True))
    a = _A_ref(x, A_k, A_v, A_shift_k, A_ssm_k, A_ssm_D)
    b = _B_ref(x, B_w_gate, B_w_up, B_w_down)
    return g * a + (1 - g) * b
```

```cpp
#include <hip/hip_runtime.h>
#include <hip/hip_bf16.h>
#include <stdint.h>

#define LAS __attribute__((address_space(3)))
#define GAS __attribute__((address_space(1)))
typedef __bf16 bf16x8 __attribute__((ext_vector_type(8)));
typedef __bf16 bf16x4 __attribute__((ext_vector_type(4)));
typedef __bf16 bf16x2 __attribute__((ext_vector_type(2)));
typedef float f32x4 __attribute__((ext_vector_type(4)));
typedef float f32x2 __attribute__((ext_vector_type(2)));
typedef unsigned u32x4 __attribute__((ext_vector_type(4)));
typedef unsigned u32x2 __attribute__((ext_vector_type(2)));
typedef LAS char lchar;
typedef const GAS char gchar;

constexpr int T_ = 2048, D_ = 512, F_ = 1792;
constexpr size_t WS_FLAGS = 0;
constexpr size_t WS_GATE  = 65536;
constexpr size_t WS_KVT   = 131072;
constexpr size_t WS_Y2T   = WS_KVT + (size_t)D_ * T_ * 2;
constexpr size_t WS_H     = WS_Y2T + (size_t)D_ * T_ * 2;

struct Params {
    const char* q; const char* k; const char* v; const char* shift_k; const char* ssm_k; const char* ssm_D;
    const char* wg; const char* wu; const char* wd; const char* gate_w;
    char* out; char* ws;
};

#define WAIT_V(n) asm volatile("s_waitcnt vmcnt(" #n ")" ::: "memory")
#define WAIT_L(n) asm volatile("s_waitcnt lgkmcnt(" #n ")" ::: "memory")
#define BAR() __builtin_amdgcn_s_barrier()
#define SCHED() __builtin_amdgcn_sched_barrier(0)
#define FENCE() asm volatile("" ::: "memory")

__device__ __forceinline__ void dma16(const char* g, unsigned voff, lchar* l) {
    __builtin_amdgcn_global_load_lds((const GAS void*)(g + voff), (LAS void*)l, 16, 0, 0);
}
__device__ __forceinline__ bf16x8 lds_b128(lchar* p) { return *(LAS bf16x8*)p; }
__device__ __forceinline__ bf16x8 lds_tr2(lchar* p0, lchar* p1) {
    bf16x4 a = __builtin_amdgcn_ds_read_tr16_b64_v4bf16((LAS bf16x4*)p0);
    bf16x4 b = __builtin_amdgcn_ds_read_tr16_b64_v4bf16((LAS bf16x4*)p1);
    return __builtin_shufflevector(a, b, 0, 1, 2, 3, 4, 5, 6, 7);
}
__device__ __forceinline__ f32x4 mfma(bf16x8 a, bf16x8 b, f32x4 c) { return __builtin_amdgcn_mfma_f32_16x16x32_bf16(a, b, c, 0, 0, 0); }
__device__ __forceinline__ unsigned pk_bf16(float lo, float hi) {
    bf16x2 r; r[0] = (__bf16)lo; r[1] = (__bf16)hi; return __builtin_bit_cast(unsigned, r);
}
__device__ __forceinline__ float bf_lo(unsigned u) { return __builtin_bit_cast(float, u << 16); }
__device__ __forceinline__ float bf_hi(unsigned u) { return __builtin_bit_cast(float, u & 0xffff0000u); }
__device__ __forceinline__ u32x4 gload16(const char* p) { return *(const GAS u32x4*)p; }

constexpr int G1_STAGE = 49152;
__device__ __forceinline__ float geglu(float g, float u) {
    const float k0 = -2.302208198f, k1 = -0.1029432397f;
    float e = __builtin_amdgcn_exp2f(g * (k0 + k1 * g * g));
    return g * u * __builtin_amdgcn_rcpf(1.0f + e);
}
__device__ __forceinline__ void gemm1_phase(const Params& p, lchar* lds, int mt, int ft, int wid, int lane) {
    const int wm = wid >> 1, wn = wid & 1;
    const int c = lane & 15, q = lane >> 4, qq = (lane & 15) >> 2, pp = lane & 3;
    const unsigned xoff = (lane >> 3) * 1024 + (((lane & 7) ^ (lane >> 3)) << 4);
    const unsigned woff0 = (lane >> 4) * (F_ * 2) + (((lane & 15) ^ (2 * (lane >> 4))) << 4);
    const unsigned woff1 = woff0 ^ 128;
    const char* xb = p.q + (size_t)(128 * mt + 32 * wid) * 1024;
    const char* gb = p.wg + (size_t)(16 * wid) * (F_ * 2) + ft * 256;
    const char* ub = p.wu + (size_t)(16 * wid) * (F_ * 2) + ft * 256;
    auto stage = [&](int kt, int buf) {
        lchar* s = lds + buf * G1_STAGE;
#pragma unroll
        for (int i = 0; i < 4; ++i) dma16(xb + i * 8192 + kt * 128, xoff, s + (32 * wid + 8 * i) * 128);
#pragma unroll
        for (int i = 0; i < 4; ++i) dma16(gb + (size_t)(64 * kt + 4 * i) * (F_ * 2), (i & 2) ? woff1 : woff0, s + 16384 + (16 * wid + 4 * i) * 256);
#pragma unroll
        for (int i = 0; i < 4; ++i) dma16(ub + (size_t)(64 * kt + 4 * i) * (F_ * 2), (i & 2) ? woff1 : woff0, s + 32768 + (16 * wid + 4 * i) * 256);
    };
    const int xr0 = (64 * wm + c) * 128 + ((q ^ (c & 7)) << 4);
    const int sk = qq | ((q & 1) << 2);
    int wr[4];
#pragma unroll
    for (int n = 0; n < 4; ++n) wr[n] = 16384 + (8 * q + qq) * 256 + (((4 * wn + n) ^ sk) << 5) + ((pp >> 1) << 4) + ((pp & 1) << 3);

    f32x4 ag[4][4], au[4][4];
#pragma unroll
    for (int m = 0; m < 4; ++m)
#pragma unroll
        for (int n = 0; n < 4; ++n) { ag[m][n] = (f32x4){0, 0, 0, 0}; au[m][n] = (f32x4){0, 0, 0, 0}; }

    bf16x8 xa[4], ga[4], ua[4], xb2[4], gb2[4], ub2[4];
    auto rd = [&](lchar* s, int ss, bf16x8* xf, bf16x8* gf, bf16x8* uf) {
#pragma unroll
        for (int n = 0; n < 4; ++n) {
            lchar* a = s + wr[n] + ss * 8192;
            gf[n] = lds_tr2(a, a + 1024);
        }
#pragma unroll
        for (int m = 0; m < 4; ++m) xf[m] = lds_b128(s + (xr0 ^ (ss * 64)) + m * 2048);
#pragma unroll
        for (int n = 0; n < 4; ++n) {
            lchar* a = s + wr[n] + ss * 8192 + 16384;
            uf[n] = lds_tr2(a, a + 1024);
        }
    };
    auto mm = [&](bf16x8* xf, bf16x8* gf, bf16x8* uf) {
#pragma unroll
        for (int n = 0; n < 4; ++n)
#pragma unroll
            for (int m = 0; m < 4; ++m) ag[m][n] = mfma(gf[n], xf[m], ag[m][n]);
#pragma unroll
        for (int n = 0; n < 4; ++n)
#pragma unroll
            for (int m = 0; m < 4; ++m) au[m][n] = mfma(uf[n], xf[m], au[m][n]);
    };

    stage(0, 0); stage(1, 1);
    WAIT_V(12); BAR(); FENCE();
    stage(2, 2);
    rd(lds, 0, xa, ga, ua);
    rd(lds, 1, xb2, gb2, ub2);
    mm(xa, ga, ua);
#pragma unroll
    for (int kt = 1; kt < 8; ++kt) {
        lchar* s = lds + (kt % 3) * G1_STAGE;
        if (kt < 7) WAIT_V(12); else WAIT_V(0);
        WAIT_L(0); BAR(); FENCE();
        if (kt + 2 < 8) stage(kt + 2, (kt + 2) % 3);
        rd(s, 0, xa, ga, ua);
        mm(xb2, gb2, ub2);
        rd(s, 1, xb2, gb2, ub2);
        mm(xa, ga, ua);
    }
    mm(xb2, gb2, ub2);

    char* hb = p.ws + WS_H + ((size_t)((2 * mt + wm) * 56 + 4 * ft + 2 * wn) * 4) * 1024 + lane * 16;
#pragma unroll
    for (int j = 0; j < 2; ++j)
#pragma unroll
        for (int m = 0; m < 4; ++m) {
            u32x4 o;
            o.x = pk_bf16(geglu(ag[m][2 * j][0], au[m][2 * j][0]), geglu(ag[m][2 * j][1], au[m][2 * j][1]));
            o.y = pk_bf16(geglu(ag[m][2 * j][2], au[m][2 * j][2]), geglu(ag[m][2 * j][3], au[m][2 * j][3]));
            o.z = pk_bf16(geglu(ag[m][2 * j + 1][0], au[m][2 * j + 1][0]), geglu(ag[m][2 * j + 1][1], au[m][2 * j + 1][1]));
            o.w = pk_bf16(geglu(ag[m][2 * j + 1][2], au[m][2 * j + 1][2]), geglu(ag[m][2 * j + 1][3], au[m][2 * j + 1][3]));
            *(GAS u32x4*)(hb + (j * 4 + m) * 1024) = o;
        }
}

__device__ __forceinline__ void prep_phase(const Params& p, lchar* lds, int tb, int wid, int lane) {
    const int tg = lane >> 3, cc = lane & 7;
    const int t0 = 64 * tb + 8 * tg;
#pragma unroll 1
    for (int it = 0; it < 2; ++it) {
        const int cg = wid + 4 * it;
        const int ch0 = 64 * cg + 8 * cc;
        u32x4 kr[11], vr[8], sr[4];
#pragma unroll
        for (int i = 0; i < 11; ++i) {
            const int t = t0 - 3 + i;
            kr[i] = (t >= 0) ? gload16(p.k + (size_t)t * 1024 + ch0 * 2) : (u32x4){0, 0, 0, 0};
        }
#pragma unroll
        for (int i = 0; i < 8; ++i) vr[i] = gload16(p.v + (size_t)(t0 + i) * 1024 + ch0 * 2);
#pragma unroll
        for (int i = 0; i < 4; ++i) sr[i] = gload16(p.shift_k + ch0 * 8 + i * 16);
#pragma unroll
        for (int i = 0; i < 8; ++i) {
            const unsigned s01 = sr[i >> 1][(i & 1) * 2], s23 = sr[i >> 1][(i & 1) * 2 + 1];
            const float s0 = bf_lo(s01), s1 = bf_hi(s01), s2 = bf_lo(s23), s3 = bf_hi(s23);
            float kk[11];
#pragma unroll
            for (int r = 0; r < 11; ++r) kk[r] = (i & 1) ? bf_hi(kr[r][i >> 1]) : bf_lo(kr[r][i >> 1]);
            float kv[8];
#pragma unroll
            for (int r = 0; r < 8; ++r) {
                const float vv = (i & 1) ? bf_hi(vr[r][i >> 1]) : bf_lo(vr[r][i >> 1]);
                const float ks = kk[r] * s3 + kk[r + 1] * s2 + kk[r + 2] * s1 + kk[r + 3] * s0;
                kv[r] = ks * vv;
            }
            u32x4 o;
            o.x = pk_bf16(kv[7], kv[6]); o.y = pk_bf16(kv[5], kv[4]); o.z = pk_bf16(kv[3], kv[2]); o.w = pk_bf16(kv[1], kv[0]);
            const int ch = ch0 + i;
            *(LAS u32x4*)(lds + ch * 128 + ((tg ^ cc) << 4)) = o;
        }
    }
    {
        const int i16 = lane & 15, tk = lane >> 4;
        float gw[32];
#pragma unroll
        for (int j = 0; j < 4; ++j) {
            const u32x4 w = gload16(p.gate_w + (i16 + 16 * j) * 16);
#pragma unroll
            for (int e = 0; e < 4; ++e) { gw[j * 8 + 2 * e] = bf_lo(w[e]); gw[j * 8 + 2 * e + 1] = bf_hi(w[e]); }
        }
#pragma unroll
        for (int g4 = 0; g4 < 4; ++g4) {
            const int t = 64 * tb + 16 * wid + 4 * g4 + tk;
            float acc = 0.f;
#pragma unroll
            for (int j = 0; j < 4; ++j) {
                const u32x4 xv = gload16(p.q + (size_t)t * 1024 + (i16 + 16 * j) * 16);
#pragma unroll
                for (int e = 0; e < 4; ++e) { acc += bf_lo(xv[e]) * gw[j * 8 + 2 * e]; acc += bf_hi(xv[e]) * gw[j * 8 + 2 * e + 1]; }
            }
            acc += __shfl_xor(acc, 1); acc += __shfl_xor(acc, 2); acc += __shfl_xor(acc, 4); acc += __shfl_xor(acc, 8);
            if (i16 == 0) *(GAS float*)(p.ws + WS_GATE + t * 4) = __builtin_amdgcn_rcpf(1.0f + __builtin_amdgcn_exp2f(-1.4426950408889634f * acc));
        }
    }
    WAIT_L(0); BAR(); FENCE();
#pragma unroll
    for (int i = 0; i < 16; ++i) {
        const int ch = 128 * wid + 8 * i + (lane >> 3);
        const int cs = (ch >> 3) & 7;
        const u32x4 o = *(LAS u32x4*)(lds + ch * 128 + (((lane & 7) ^ cs) << 4));
        *(GAS u32x4*)(p.ws + WS_KVT + (size_t)ch * 4096 + tb * 128 + (lane & 7) * 16) = o;
    }
}

constexpr int CV_CP = 4160;
constexpr int CV_X = 4 * CV_CP;
constexpr int CV_CH = CV_X + 4608;
constexpr int CV_RED = 2 * CV_CH;
constexpr int CV_END = CV_RED + 16384;
__device__ __forceinline__ void conv_phase(const Params& p, lchar* lds, int cp, int wid, int lane) {
    const int tid = wid * 64 + lane;
#pragma unroll
    for (int c2 = 0; c2 < 2; ++c2) {
        const int ch = 2 * cp + c2;
        lchar* base = lds + c2 * CV_CH;
        const u32x4 xv = gload16(p.ws + WS_KVT + (size_t)ch * 4096 + tid * 16);
        const u32x4 h0 = gload16(p.ssm_k + (size_t)ch * 4096 + tid * 16);
        const u32x4 h1 = (tid < 255) ? gload16(p.ssm_k + (size_t)ch * 4096 + tid * 16 + 16) : (u32x4){0, 0, 0, 0};
        *(LAS u32x4*)(base + CV_X + 512 + tid * 16) = xv;
        if (tid < 32) *(LAS u32x4*)(base + CV_X + tid * 16) = (u32x4){0, 0, 0, 0};
        auto emit = [&](int chunk, u32x4 a, u32x4 b) {
            u32x4 o;
            o = (u32x4){__builtin_amdgcn_alignbit(a.y, a.x, 16), __builtin_amdgcn_alignbit(a.z, a.y, 16), __builtin_amdgcn_alignbit(a.w, a.z, 16), __builtin_amdgcn_alignbit(b.x, a.w, 16)};
            *(LAS u32x4*)(base + 0 * CV_CP + chunk * 16) = o;
            o = (u32x4){a.y, a.z, a.w, b.x};
            *(LAS u32x4*)(base + 1 * CV_CP + chunk * 16) = o;
            o = (u32x4){__builtin_amdgcn_alignbit(a.z, a.y, 16), __builtin_amdgcn_alignbit(a.w, a.z, 16), __builtin_amdgcn_alignbit(b.x, a.w, 16), __builtin_amdgcn_alignbit(b.y, b.x, 16)};
            *(LAS u32x4*)(base + 2 * CV_CP + chunk * 16) = o;
            o = (u32x4){a.z, a.w, b.x, b.y};
            *(LAS u32x4*)(base + 3 * CV_CP + chunk * 16) = o;
        };
        emit(tid + 2, h0, h1);
        if (tid < 2) {
            const u32x4 z = {0, 0, 0, 0};
            emit(1 - tid, z, tid == 0 ? h0 : z);
        }
    }
    WAIT_L(0); BAR(); FENCE();
    const int c2 = wid >> 1, half = wid & 1;
    lchar* base = lds + c2 * CV_CH;
    const int r = lane & 15, q = lane >> 4;
    lchar* ap = base + (r & 3) * CV_CP + ((r >> 2) * 4 - 8 * q + 24) * 2;
    lchar* bp = base + CV_X + 512 + (16 * r - 16 + 8 * q) * 2;
    f32x4 acc[8];
#pragma unroll
    for (int g = 0; g < 8; ++g) acc[g] = (f32x4){0, 0, 0, 0};
#pragma unroll 1
    for (int ww = 0; ww < 4; ++ww) {
        const int w = 4 * half + ww;
        lchar* a = ap + 64 * w;
        lchar* b = bp - 64 * w;
        bf16x8 A[8], B[8];
#pragma unroll
        for (int u = 0; u < 8; ++u) {
            bf16x4 lo = *(LAS bf16x4*)(a + 512 * u), hi = *(LAS bf16x4*)(a + 512 * u + 8);
            A[u] = __builtin_shufflevector(lo, hi, 0, 1, 2, 3, 4, 5, 6, 7);
            B[u] = lds_b128(b + 512 * u);
        }
#pragma unroll
        for (int u = 0; u < 8; ++u)
#pragma unroll
            for (int t = 0; t + u < 8; ++t) acc[u + t] = mfma(A[u], B[t], acc[u + t]);
    }
    lchar* red = lds + CV_RED;
#pragma unroll
    for (int j = 0; j < 4; ++j) {
        f32x4 a = acc[j], b = acc[4 + j];
        asm volatile("" : "+v"(a), "+v"(b));
        acc[j] = half ? b : a; acc[4 + j] = half ? a : b;
        *(LAS f32x4*)(red + wid * 4096 + j * 1024 + lane * 16) = acc[4 + j];
    }
    WAIT_L(0); BAR(); FENCE();
    const int ch = 2 * cp + c2;
    const float dch = bf_lo((unsigned)*(const GAS unsigned short*)(p.ssm_D + ch * 2));
    f32x4 fin[4];
#pragma unroll
    for (int j = 0; j < 4; ++j) {
        const f32x4 o = *(LAS f32x4*)(red + (wid ^ 1) * 4096 + j * 1024 + lane * 16);
        fin[j] = acc[j] + o;
    }
    WAIT_L(0); BAR(); FENCE();
#pragma unroll
    for (int j = 0; j < 4; ++j) {
        const int t0 = 256 * (4 * half + j) + 16 * r + 4 * q;
        const u32x2 kv = *(LAS u32x2*)(base + CV_X + 512 + ((t0 & ~7) + 4 - (t0 & 7)) * 2);
        const float y0 = fin[j][0] + bf_hi(kv.y) * dch, y1 = fin[j][1] + bf_lo(kv.y) * dch;
        const float y2 = fin[j][2] + bf_hi(kv.x) * dch, y3 = fin[j][3] + bf_lo(kv.x) * dch;
        u32x2 o; o.x = pk_bf16(y0, y1); o.y = pk_bf16(y2, y3);
        *(LAS u32x2*)(red + c2 * 4096 + t0 * 2) = o;
    }
    WAIT_L(0); BAR(); FENCE();
    {
        const int cc = tid >> 7, off = (tid & 127) * 32;
        const u32x4 o0 = *(LAS u32x4*)(red + cc * 4096 + off), o1 = *(LAS u32x4*)(red + cc * 4096 + off + 16);
        char* y = p.ws + WS_Y2T + (size_t)(2 * cp + cc) * 4096 + off;
        *(GAS u32x4*)y = o0; *(GAS u32x4*)(y + 16) = o1;
    }
}

constexpr int G2_NS = 6;
constexpr int G2_RING = G2_NS * 4096;
constexpr int G2_RED = 0;
constexpr int G2_Y = 4 * G2_RING > 65536 ? 4 * G2_RING : 65536;
constexpr int G2_Q = G2_Y + 8192;
constexpr int G2_O = G2_Q + 8192;
constexpr int G2_END = G2_O + 8192;
__device__ __forceinline__ void gemm2_phase(const Params& p, lchar* lds, int tb, int cg, int wid, int lane) {
    const int c = lane & 15, q = lane >> 4, qq = (lane & 15) >> 2, pp = lane & 3;
    lchar* ring = lds + wid * G2_RING;
    const unsigned doff = (lane >> 3) * 1024 + (((lane & 7) ^ (2 * ((lane >> 4) & 3))) << 4);
    const char* wb = p.wd + (size_t)(14 * wid * 32) * 1024 + cg * 128;
    const char* hb = p.ws + WS_H + ((size_t)(tb * 56 + 14 * wid) * 4) * 1024 + lane * 16;
    auto stage_w = [&](int s) {
        lchar* d = ring + (s % G2_NS) * 4096;
#pragma unroll
        for (int i = 0; i < 4; ++i) dma16(wb + (size_t)(32 * s + 8 * i) * 1024, doff, d + i * 1024);
    };
    {
#pragma unroll
        for (int i = 0; i < 2; ++i) {
            const int row = 16 * wid + 8 * i + (lane >> 3);
            const u32x4 yv = gload16(p.ws + WS_Y2T + (size_t)(64 * cg + row) * 4096 + tb * 128 + (lane & 7) * 16);
            *(LAS u32x4*)(lds + G2_Y + row * 128 + (lane & 7) * 16) = yv;
            const u32x4 qv = gload16(p.q + (size_t)(64 * tb + row) * 1024 + cg * 128 + (lane & 7) * 16);
            *(LAS u32x4*)(lds + G2_Q + row * 128 + (((lane & 7) ^ (row & 7)) << 4)) = qv;
        }
    }
    const float gate = *(const GAS float*)(p.ws + WS_GATE + (64 * tb + 16 * wid + c) * 4);

    const int sw = 2 * (q & 1) + (qq >> 1);
    int ar[4];
#pragma unroll
    for (int n = 0; n < 4; ++n) ar[n] = (4 * q + qq) * 128 + ((n ^ sw) << 5) + ((pp >> 1) << 4) + ((pp & 1) << 3);
    f32x4 acc[4][4];
#pragma unroll
    for (int n = 0; n < 4; ++n)
#pragma unroll
        for (int m = 0; m < 4; ++m) acc[n][m] = (f32x4){0, 0, 0, 0};

    constexpr int HD = 3;
    bf16x8 hf[HD][4];
    FENCE();
#pragma unroll
    for (int s = 0; s < G2_NS - 1; ++s) stage_w(s);
#pragma unroll
    for (int s = 0; s < HD - 1; ++s)
#pragma unroll
        for (int m = 0; m < 4; ++m) hf[s][m] = *(const GAS bf16x8*)(hb + (s * 4 + m) * 1024);
#pragma unroll
    for (int s = 0; s < 14; ++s) {
        if (s + G2_NS - 1 < 14) stage_w(s + G2_NS - 1);
        if (s + HD - 1 < 14) {
#pragma unroll
            for (int m = 0; m < 4; ++m) hf[(s + HD - 1) % HD][m] = *(const GAS bf16x8*)(hb + ((s + HD - 1) * 4 + m) * 1024);
        }
        WAIT_V(16);
        if (s >= 14 - 5) WAIT_V(0);
        lchar* d = ring + (s % G2_NS) * 4096;
        bf16x8 A[4];
#pragma unroll
        for (int n = 0; n < 4; ++n) A[n] = lds_tr2(d + ar[n], d + ar[n] + 2048);
#pragma unroll
        for (int n = 0; n < 4; ++n)
#pragma unroll
            for (int m = 0; m < 4; ++m) acc[n][m] = mfma(A[n], hf[s % HD][m], acc[n][m]);
        WAIT_L(0);
        SCHED();
    }
    WAIT_V(0); WAIT_L(0); BAR(); FENCE();
    lchar* red = lds + G2_RED;
#pragma unroll
    for (int m = 0; m < 4; ++m)
#pragma unroll
        for (int n = 0; n < 4; ++n)
            *(LAS f32x4*)(red + wid * 16384 + (16 * m + c) * 256 + (((4 * n + q) ^ c) << 4)) = acc[n][m];
    WAIT_L(0); BAR(); FENCE();
    const int tok = 16 * wid + c;
#pragma unroll
    for (int ps = 0; ps < 2; ++ps) {
        const int d0 = 32 * ps + 8 * q;
        f32x4 b0 = {0, 0, 0, 0}, b1 = {0, 0, 0, 0};
#pragma unroll
        for (int w2 = 0; w2 < 4; ++w2) {
            b0 += *(LAS f32x4*)(red + w2 * 16384 + tok * 256 + ((((d0 >> 2)) ^ c) << 4));
            b1 += *(LAS f32x4*)(red + w2 * 16384 + tok * 256 + ((((d0 >> 2) + 1) ^ c) << 4));
        }
        lchar* ya = lds + G2_Y + (d0 + qq) * 128 + (16 * wid + 4 * pp) * 2;
        const u32x4 yv = __builtin_bit_cast(u32x4, lds_tr2(ya, ya + 512));
        const u32x4 qv = *(LAS u32x4*)(lds + G2_Q + tok * 128 + (((d0 >> 3) ^ (tok & 7)) << 4));
        const float g1 = gate, g0 = 1.0f - gate;
        u32x4 o;
        o.x = pk_bf16(g1 * bf_lo(qv.x) * bf_lo(yv.x) + g0 * b0[0], g1 * bf_hi(qv.x) * bf_hi(yv.x) + g0 * b0[1]);
        o.y = pk_bf16(g1 * bf_lo(qv.y) * bf_lo(yv.y) + g0 * b0[2], g1 * bf_hi(qv.y) * bf_hi(yv.y) + g0 * b0[3]);
        o.z = pk_bf16(g1 * bf_lo(qv.z) * bf_lo(yv.z) + g0 * b1[0], g1 * bf_hi(qv.z) * bf_hi(yv.z) + g0 * b1[1]);
        o.w = pk_bf16(g1 * bf_lo(qv.w) * bf_lo(yv.w) + g0 * b1[2], g1 * bf_hi(qv.w) * bf_hi(yv.w) + g0 * b1[3]);
        *(LAS u32x4*)(lds + G2_O + tok * 128 + (((d0 >> 3) ^ (tok & 7)) << 4)) = o;
    }
    WAIT_L(0); FENCE();
#pragma unroll
    for (int i = 0; i < 2; ++i) {
        const int row = 16 * wid + 8 * i + (lane >> 3);
        const u32x4 o = *(LAS u32x4*)(lds + G2_O + row * 128 + (((lane & 7) ^ (row & 7)) << 4));
        *(GAS u32x4*)(p.out + (size_t)(64 * tb + row) * 1024 + cg * 128 + (lane & 7) * 16) = o;
    }
}

__global__ void __launch_bounds__(256, 1) k_a(Params p) {
    extern __shared__ __attribute__((aligned(16))) char smem[];
    lchar* lds = (lchar*)smem;
    const int wid = __builtin_amdgcn_readfirstlane(threadIdx.x >> 6), lane = threadIdx.x & 63;
    const int xcd = blockIdx.x & 7, slot = blockIdx.x >> 3;
    if (slot < 28) gemm1_phase(p, lds, 4 * (xcd >> 1) + (slot & 3), 7 * (xcd & 1) + (slot >> 2), wid, lane);
    else prep_phase(p, lds, xcd * 4 + (slot - 28), wid, lane);
}
__global__ void __launch_bounds__(256, 1) k_b(Params p) {
    extern __shared__ __attribute__((aligned(16))) char smem[];
    lchar* lds = (lchar*)smem;
    const int wid = __builtin_amdgcn_readfirstlane(threadIdx.x >> 6), lane = threadIdx.x & 63;
    conv_phase(p, lds, blockIdx.x, wid, lane);
}
__global__ void __launch_bounds__(256, 1) k_c(Params p) {
    extern __shared__ __attribute__((aligned(16))) char smem[];
    lchar* lds = (lchar*)smem;
    const int wid = __builtin_amdgcn_readfirstlane(threadIdx.x >> 6), lane = threadIdx.x & 63;
    const int xcd = blockIdx.x & 7, slot = blockIdx.x >> 3;
    gemm2_phase(p, lds, 4 * xcd + (slot >> 3), slot & 7, wid, lane);
}

extern "C" void kernel_launch(void* const* d_in, const int* in_sizes, int n_in, void* d_out, int out_size, void* d_ws, size_t ws_size, hipStream_t stream) {
    Params p;
    p.q = (const char*)d_in[0]; p.k = (const char*)d_in[1]; p.v = (const char*)d_in[2]; p.shift_k = (const char*)d_in[3];
    p.ssm_k = (const char*)d_in[4]; p.ssm_D = (const char*)d_in[5];
    p.wg = (const char*)d_in[7]; p.wu = (const char*)d_in[8]; p.wd = (const char*)d_in[9]; p.gate_w = (const char*)d_in[10];
    p.out = (char*)d_out; p.ws = (char*)d_ws;
    hipFuncSetAttribute((const void*)k_a, hipFuncAttributeMaxDynamicSharedMemorySize, 3 * G1_STAGE);
    hipFuncSetAttribute((const void*)k_b, hipFuncAttributeMaxDynamicSharedMemorySize, CV_END);
    hipFuncSetAttribute((const void*)k_c, hipFuncAttributeMaxDynamicSharedMemorySize, G2_END);
    k_a<<<256, 256, 3 * G1_STAGE, stream>>>(p);
    k_b<<<256, 256, CV_END, stream>>>(p);
    k_c<<<256, 256, G2_END, stream>>>(p);
}
```

```cpp
#include <hip/hip_runtime.h>
#include <hip/hip_bf16.h>
#include <stdint.h>
#include <type_traits>

#define LAS __attribute__((address_space(3)))
#define GAS __attribute__((address_space(1)))
#define DI __device__ __forceinline__
typedef __bf16 bf16x8 __attribute__((ext_vector_type(8)));
typedef __bf16 bf16x4 __attribute__((ext_vector_type(4)));
typedef __bf16 bf16x2 __attribute__((ext_vector_type(2)));
typedef float f32x4 __attribute__((ext_vector_type(4)));
typedef float f32x2 __attribute__((ext_vector_type(2)));
typedef unsigned u32x4 __attribute__((ext_vector_type(4)));
typedef unsigned u32x2 __attribute__((ext_vector_type(2)));
typedef LAS char lchar;
typedef __amdgpu_buffer_rsrc_t rsrc_t;
typedef GAS unsigned gu32;

constexpr int T_ = 2048, D_ = 512, F_ = 1792;
constexpr unsigned WS_EPOCH = 0;
constexpr unsigned WS_HFLAG = 1024;
constexpr unsigned WS_KVFLAG = 8192;
constexpr unsigned WS_YFLAG = 12288;
constexpr unsigned WS_GFLAG = 20480;
constexpr unsigned WS_STAMP = 32768;
constexpr unsigned WS_GATE = 65536;
constexpr unsigned WS_KVT = 131072;
constexpr unsigned WS_Y2T = WS_KVT + D_ * T_ * 2;
constexpr unsigned WS_H = WS_Y2T + D_ * T_ * 2;
constexpr unsigned WS_END = WS_H + T_ * F_ * 2;
constexpr int G1_STAGE = 49152;
constexpr int G2_NS = 6, G2_RING = G2_NS * 4096;
constexpr int L_OST = 65536;
constexpr int CV_CP = 4160, CV_X = 4 * CV_CP, CV_CH = CV_X + 4608;
constexpr int L_CV = 98304;
constexpr int L_EX = L_CV + 2 * CV_CH;
constexpr int L_SCR = L_EX + 16384;
constexpr int L_TAG = L_SCR + 256;
constexpr int L_CNTA = L_TAG + 4, L_CNTB = L_TAG + 8, L_PAIR = L_TAG + 12, L_HRDY = L_TAG + 28, L_KVRDY = L_TAG + 32, L_YRDY = L_TAG + 36, L_RING = L_TAG + 40, L_LATE = L_TAG + 44, L_CNTP = L_TAG + 48;
constexpr int L_PSK = L_TAG + 64;
constexpr int LDS_BYTES = L_PSK + 4096;
static_assert(LDS_BYTES <= 163840, "lds");

struct Params {
    const char* q; const char* k; const char* v; const char* shift_k; const char* ssm_k; const char* ssm_D;
    const char* wg; const char* wu; const char* wd; const char* gate_w;
    char* out; char* ws;
};

#define WAIT_V(n) asm volatile("s_waitcnt vmcnt(" #n ")" ::: "memory")
#define WAIT_VN(n) asm volatile("s_waitcnt vmcnt(%0)" :: "i"(n) : "memory")
#define WAIT_L(n) asm volatile("s_waitcnt lgkmcnt(" #n ")" ::: "memory")
#define BAR() do { asm volatile("" ::: "memory"); __builtin_amdgcn_s_barrier(); asm volatile("" ::: "memory"); } while (0)
#define SCHED() __builtin_amdgcn_sched_barrier(0)
#define FENCE() asm volatile("" ::: "memory")

DI rsrc_t mk_rsrc(const void* p, unsigned bytes) { return __builtin_amdgcn_make_buffer_rsrc((void*)p, (short)0, (int)bytes, 0x00020000); }
template <int OFF> DI void dma_m0(unsigned m0, unsigned voff, rsrc_t r, unsigned soff) {
    asm volatile("s_mov_b32 m0, %0\n\ts_nop 0\n\tbuffer_load_dwordx4 %1, %2, %3 offen offset:%4 lds" :: "s"(m0), "v"(voff), "s"(r), "s"(soff), "i"(OFF) : "memory");
}
template <int OFF> DI void dma_c(unsigned voff, rsrc_t r, unsigned soff) {
    asm volatile("buffer_load_dwordx4 %0, %1, %2 offen offset:%3 lds" :: "v"(voff), "s"(r), "s"(soff), "i"(OFF) : "memory");
}
DI void touch(unsigned m0, unsigned voff, rsrc_t r, unsigned soff) {
    asm volatile("s_mov_b32 m0, %0\n\ts_nop 0\n\tbuffer_load_dword %1, %2, %3 offen lds" :: "s"(m0), "v"(voff), "s"(r), "s"(soff) : "memory");
}
DI bf16x8 lds_b128(lchar* p) { return *(LAS bf16x8*)p; }
DI bf16x4 lds_tr(lchar* p) { return __builtin_amdgcn_ds_read_tr16_b64_v4bf16((LAS bf16x4*)p); }
DI bf16x8 cat8(bf16x4 a, bf16x4 b) { return __builtin_shufflevector(a, b, 0, 1, 2, 3, 4, 5, 6, 7); }
DI f32x4 mfma(bf16x8 a, bf16x8 b, f32x4 c) { return __builtin_amdgcn_mfma_f32_16x16x32_bf16(a, b, c, 0, 0, 0); }
DI unsigned pk_bf16(float lo, float hi) { bf16x2 r; r[0] = (__bf16)lo; r[1] = (__bf16)hi; return __builtin_bit_cast(unsigned, r); }
DI float bf_lo(unsigned u) { return __builtin_bit_cast(float, u << 16); }
DI float bf_hi(unsigned u) { return __builtin_bit_cast(float, u & 0xffff0000u); }
DI u32x4 gload16(const char* p) { return *(const GAS u32x4*)p; }
DI unsigned rfl(unsigned x) { return __builtin_amdgcn_readfirstlane(x); }
DI u32x4 ld16_dev(rsrc_t r, unsigned voff, unsigned soff) { return __builtin_amdgcn_raw_buffer_load_b128(r, voff, soff, 16); }
DI void st16_dev(u32x4 v, rsrc_t r, unsigned voff, unsigned soff) { __builtin_amdgcn_raw_buffer_store_b128(v, r, voff + soff, 0, 16); }
DI unsigned ld4_dev(const char* p) { return __hip_atomic_load((gu32*)p, __ATOMIC_RELAXED, __HIP_MEMORY_SCOPE_AGENT); }
DI void st4_dev(char* p, unsigned v) { __hip_atomic_store((gu32*)p, v, __ATOMIC_RELAXED, __HIP_MEMORY_SCOPE_AGENT); }
DI void publish(char* flag, unsigned tag, int lane) { WAIT_V(0); if (lane == 0) st4_dev(flag, tag); }
template <int NV, int SLEEP = 4> DI void poll(const char* flags, int n, unsigned tag, int lane) {
    for (int it = 0; it < 200000; ++it) {
        bool ok = true;
#pragma unroll
        for (int j = 0; j < NV; ++j) { const int i = lane + 64 * j; if (i < n) ok &= (ld4_dev(flags + i * 4) == tag); }
        if (__all(ok)) break;
        __builtin_amdgcn_s_sleep(SLEEP);
    }
    FENCE();
}
DI void lds_arrive(lchar* w, int lane) { WAIT_L(0); if (lane == 0) __hip_atomic_fetch_add((LAS unsigned*)w, 1u, __ATOMIC_RELAXED, __HIP_MEMORY_SCOPE_WORKGROUP); }
template <int SLEEP = 1> DI void lds_wait(lchar* w, unsigned n) {
    for (int it = 0; it < 4000000; ++it) {
        if (rfl(__hip_atomic_load((LAS unsigned*)w, __ATOMIC_RELAXED, __HIP_MEMORY_SCOPE_WORKGROUP)) >= n) break;
        __builtin_amdgcn_s_sleep(SLEEP);
    }
    FENCE();
}
DI unsigned mk_tag(unsigned epoch) { return rfl(epoch) * 1664525u + 1013904223u; }
#define STAMP(i) do {} while (0)
#define STAMP_DECL
#define STAMP_ARG
#define STAMP_PASS
#define STAMP_END do {} while (0)
#define STAMPC(i) do {} while (0)

template <int CTRL> DI float dpp_add(float v) {
    return v + __builtin_bit_cast(float, __builtin_amdgcn_update_dpp(0, __builtin_bit_cast(int, v), CTRL, 0xf, 0xf, true));
}
DI f32x2 geglu2(f32x2 g, f32x2 u) {
    const float k0 = -2.302208198f, k1 = -0.1029432397f;
    f32x2 a = g * ((g * g) * k1 + k0);
    f32x2 e; e.x = __builtin_amdgcn_exp2f(a.x); e.y = __builtin_amdgcn_exp2f(a.y);
    e = e + 1.0f;
    f32x2 r; r.x = __builtin_amdgcn_rcpf(e.x); r.y = __builtin_amdgcn_rcpf(e.y);
    return (g * u) * r;
}
DI unsigned geglu_pk(f32x4 g, f32x4 u, int i) {
    f32x2 h = geglu2((f32x2){g[i], g[i + 1]}, (f32x2){u[i], u[i + 1]});
    return pk_bf16(h.x, h.y);
}

struct G1Load {
    unsigned vx[4], vw[4]; rsrc_t rx, rg, ru; unsigned ldsb; int lw, rot;
    DI void init(const Params& p, unsigned ldsb_, int mt, int ft, int lw_, int lane) {
        ldsb = ldsb_; lw = lw_;
        rot = ((mt + ft) & 1) * 4;
        rx = mk_rsrc(p.q + (size_t)128 * mt * 1024, 128 * 1024);
        rg = mk_rsrc(p.wg + ft * 256, D_ * F_ * 2 - ft * 256);
        ru = mk_rsrc(p.wu + ft * 256, D_ * F_ * 2 - ft * 256);
        const unsigned xoff = (lane >> 3) * 1024 + (((lane & 7) ^ (lane >> 3)) << 4) + lw * 4 * 8192;
        const unsigned woff = (lane >> 4) * (F_ * 2) + (((lane & 15) ^ (2 * (lane >> 4))) << 4) + lw * 16 * (F_ * 2);
#pragma unroll
        for (int i = 0; i < 4; ++i) { vx[i] = xoff + i * (8192 - 1024); vw[i] = (woff ^ ((i & 2) ? 128 : 0)) + i * (4 * F_ * 2 - 1024); }
    }
    DI void piece4(unsigned m0, const unsigned* v, rsrc_t r, unsigned so) {
        dma_m0<0>(m0, v[0], r, so); dma_c<1024>(v[1], r, so); dma_c<2048>(v[2], r, so); dma_c<3072>(v[3], r, so);
    }
    DI void stage(int kt0, int buf) {
        const int kt = (kt0 + rot) & 7;
        const unsigned b = ldsb + buf * G1_STAGE + lw * 4096;
        piece4(b, vx, rx, kt * 128);
        piece4(b + 16384, vw, rg, kt * 64 * F_ * 2);
        piece4(b + 32768, vw, ru, kt * 64 * F_ * 2);
    }
};

struct G1Comp {
    f32x4 ag[4][4], au[4][4];
    DI void kloop(lchar* lds, int wid, int lane) {
        const int wm = wid >> 1, wn = wid & 1;
        const int c = lane & 15, q = lane >> 4, qq = (lane & 15) >> 2, pp = lane & 3;
        const int xr0 = (64 * wm + c) * 128 + ((q ^ (c & 7)) << 4);
        const int sk = qq | ((q & 1) << 2);
        int wr[4];
#pragma unroll
        for (int n = 0; n < 4; ++n) wr[n] = 16384 + (8 * q + qq) * 256 + (((4 * wn + n) ^ sk) << 5) + ((pp >> 1) << 4) + ((pp & 1) << 3);
#pragma unroll
        for (int m = 0; m < 4; ++m)
#pragma unroll
            for (int n = 0; n < 4; ++n) { ag[m][n] = (f32x4){0, 0, 0, 0}; au[m][n] = (f32x4){0, 0, 0, 0}; }
        bf16x8 X[2][4]; bf16x4 Wh[2][8][2];
        auto rdx = [&](lchar* s, int ss, int m) { return lds_b128(s + (xr0 ^ (ss * 64)) + m * 2048); };
        auto rdw = [&](lchar* s, int ss, int j, int hh) { return lds_tr(s + wr[j & 3] + ss * 8192 + (j >> 2) * 16384 + hh * 1024); };
        auto substep = [&](auto CUR, bool rd, lchar* s, int ss) {
            constexpr int cur = CUR.value, nxt = cur ^ 1;
#pragma unroll
            for (int j = 0; j < 8; ++j) {
                const bf16x8 wf = cat8(Wh[cur][j][0], Wh[cur][j][1]);
#pragma unroll
                for (int m = 0; m < 4; ++m) {
                    if (j < 4) ag[m][j] = mfma(wf, X[cur][m], ag[m][j]);
                    else au[m][j - 4] = mfma(wf, X[cur][m], au[m][j - 4]);
                    SCHED();
                    const int g = 4 * j + m;
                    if (rd) {
                        if (g < 2) Wh[nxt][0][g] = rdw(s, ss, 0, g);
                        else if (g < 6) X[nxt][g - 2] = rdx(s, ss, g - 2);
                        else if (g < 20) Wh[nxt][1 + ((g - 6) >> 1)][(g - 6) & 1] = rdw(s, ss, 1 + ((g - 6) >> 1), (g - 6) & 1);
                    }
                    SCHED();
                }
            }
        };
        std::integral_constant<int, 0> I0; std::integral_constant<int, 1> I1;
        BAR();
#pragma unroll
        for (int m = 0; m < 4; ++m) X[0][m] = rdx(lds, 0, m);
#pragma unroll
        for (int j = 0; j < 8; ++j) { Wh[0][j][0] = rdw(lds, 0, j, 0); Wh[0][j][1] = rdw(lds, 0, j, 1); }
        SCHED();
        substep(I0, true, lds, 1);
#pragma unroll
        for (int kt = 1; kt < 8; ++kt) {
            lchar* s = lds + (kt % 3) * G1_STAGE;
            WAIT_L(0); BAR(); SCHED();
            substep(I1, true, s, 0);
            substep(I0, true, s, 1);
        }
        substep(I1, false, lds, 0);
    }
    DI void epilogue(const Params& p, int mt, int ft, int wid, int lane) {
        const int wm = wid >> 1, wn = wid & 1;
        const rsrc_t rh = mk_rsrc(p.ws + WS_H, T_ * F_ * 2);
        const unsigned hb = (unsigned)((2 * mt + wm) * 56 + 4 * ft + 2 * wn) * 4096;
#pragma unroll
        for (int j = 0; j < 2; ++j)
#pragma unroll
            for (int m = 0; m < 4; ++m) {
                u32x4 o;
                o.x = geglu_pk(ag[m][2 * j], au[m][2 * j], 0); o.y = geglu_pk(ag[m][2 * j], au[m][2 * j], 2);
                o.z = geglu_pk(ag[m][2 * j + 1], au[m][2 * j + 1], 0); o.w = geglu_pk(ag[m][2 * j + 1], au[m][2 * j + 1], 2);
                st16_dev(o, rh, lane * 16, hb + (j * 4 + m) * 1024);
            }
    }
};

DI unsigned prep_phase(const Params& p, lchar* lds, int tb, int wid, int lane STAMP_ARG) {
    const int tg = lane >> 3, cc = lane & 7;
    const int t0 = 64 * tb + 8 * tg;
    const int ch0 = 64 * wid + 8 * cc;
    u32x4 kr[11], vr[8], sr[4];
    const rsrc_t rkin = mk_rsrc(p.k, T_ * D_ * 2), rvin = mk_rsrc(p.v, T_ * D_ * 2), rs = mk_rsrc(p.shift_k + 512 * wid, 512);
    const u32x4 sk = __builtin_amdgcn_raw_buffer_load_b128(rs, (lane & 31) * 16, 0, 0);
#pragma unroll
    for (int i = 0; i < 11; ++i) kr[i] = __builtin_amdgcn_raw_buffer_load_b128(rkin, (unsigned)((t0 - 3 + i) * 1024 + ch0 * 2), 0, 0);
#pragma unroll
    for (int i = 0; i < 8; ++i) vr[i] = __builtin_amdgcn_raw_buffer_load_b128(rvin, (unsigned)((t0 + i) * 1024 + ch0 * 2), 0, 0);
    const unsigned epoch = ld4_dev(p.ws + WS_EPOCH);
    lchar* st = lds + wid * 8192;
    {
        lchar* s = lds + L_PSK + wid * 512;
        *(LAS u32x4*)(s + (lane & 31) * 16) = sk;
        WAIT_L(0);
#pragma unroll
        for (int i = 0; i < 4; ++i) sr[i] = *(LAS u32x4*)(s + cc * 64 + i * 16);
    }
#pragma unroll
    for (int j = 0; j < 4; ++j) {
        const f32x2 s0 = {bf_lo(sr[j][0]), bf_lo(sr[j][2])}, s1 = {bf_hi(sr[j][0]), bf_hi(sr[j][2])};
        const f32x2 s2 = {bf_lo(sr[j][1]), bf_lo(sr[j][3])}, s3 = {bf_hi(sr[j][1]), bf_hi(sr[j][3])};
        f32x2 kk[11], kv[8], vv[8];
#pragma unroll
        for (int r = 0; r < 11; ++r) kk[r] = (f32x2){bf_lo(kr[r][j]), bf_hi(kr[r][j])};
        SCHED();
#pragma unroll
        for (int r = 0; r < 8; ++r) kv[r] = kk[r] * s3;
        SCHED();
#pragma unroll
        for (int r = 0; r < 8; ++r) kv[r] = kk[r + 1] * s2 + kv[r];
        SCHED();
#pragma unroll
        for (int r = 0; r < 8; ++r) { kv[r] = kk[r + 2] * s1 + kv[r]; vv[r] = (f32x2){bf_lo(vr[r][j]), bf_hi(vr[r][j])}; }
        SCHED();
#pragma unroll
        for (int r = 0; r < 8; ++r) kv[r] = kk[r + 3] * s0 + kv[r];
        SCHED();
#pragma unroll
        for (int r = 0; r < 8; ++r) kv[r] = kv[r] * vv[r];
        SCHED();
        u32x4 o0, o1;
        o0.x = pk_bf16(kv[7].x, kv[6].x); o0.y = pk_bf16(kv[5].x, kv[4].x); o0.z = pk_bf16(kv[3].x, kv[2].x); o0.w = pk_bf16(kv[1].x, kv[0].x);
        o1.x = pk_bf16(kv[7].y, kv[6].y); o1.y = pk_bf16(kv[5].y, kv[4].y); o1.z = pk_bf16(kv[3].y, kv[2].y); o1.w = pk_bf16(kv[1].y, kv[0].y);
        *(LAS u32x4*)(st + (8 * cc + 2 * j) * 128 + ((tg ^ cc) << 4)) = o0;
        *(LAS u32x4*)(st + (8 * cc + 2 * j + 1) * 128 + ((tg ^ cc) << 4)) = o1;
    }
    WAIT_L(0);
    STAMP(12);
    const rsrc_t rk = mk_rsrc(p.ws + WS_KVT, D_ * T_ * 2);
#pragma unroll
    for (int i = 0; i < 8; ++i) {
        const int cl = 8 * i + (lane >> 3);
        const u32x4 o = *(LAS u32x4*)(st + cl * 128 + (((lane & 7) ^ i) << 4));
        st16_dev(o, rk, (64 * wid + cl) * 4096 + (lane & 7) * 16, tb * 128);
    }
    const unsigned tag = mk_tag(epoch);
    STAMP(13);
    publish(p.ws + WS_KVFLAG + (wid * 32 + tb) * 4, tag, lane);
    STAMP(14);
    const int i16 = lane & 15, tk = lane >> 4;
    u32x4 gw[4], xv[2][4];
    const rsrc_t rq = mk_rsrc(p.q, T_ * D_ * 2), rw = mk_rsrc(p.gate_w, D_ * 2);
#pragma unroll
    for (int j = 0; j < 4; ++j) gw[j] = __builtin_amdgcn_raw_buffer_load_b128(rw, (i16 + 16 * j) * 16, 0, 0);
#pragma unroll
    for (int g4 = 0; g4 < 2; ++g4)
#pragma unroll
        for (int j = 0; j < 4; ++j) xv[g4][j] = __builtin_amdgcn_raw_buffer_load_b128(rq, (64 * tb + 8 * wid + 4 * g4 + tk) * 1024 + (i16 + 16 * j) * 16, 0, 0);
#pragma unroll
    for (int g4 = 0; g4 < 2; ++g4) {
        f32x2 acc = {0.f, 0.f};
#pragma unroll
        for (int j = 0; j < 4; ++j)
#pragma unroll
            for (int e = 0; e < 4; ++e) acc += (f32x2){bf_lo(xv[g4][j][e]), bf_hi(xv[g4][j][e])} * (f32x2){bf_lo(gw[j][e]), bf_hi(gw[j][e])};
        float a = acc.x + acc.y;
        a = dpp_add<0x111>(a); a = dpp_add<0x112>(a); a = dpp_add<0x114>(a); a = dpp_add<0x118>(a);
        const float g = __builtin_amdgcn_rcpf(1.0f + __builtin_amdgcn_exp2f(-1.4426950408889634f * a));
        if (i16 == 15) st4_dev(p.ws + WS_GATE + (64 * tb + 8 * wid + 4 * g4 + tk) * 4, __builtin_bit_cast(unsigned, g));
    }
    publish(p.ws + WS_GFLAG + (tb * 8 + wid) * 4, tag, lane);
    return tag;
}

struct G2W { rsrc_t rw; unsigned vd[4]; unsigned ldsb; };
DI void g2w_init(G2W& w, const Params& p, unsigned ldsb, int cg, int lane) {
    w.rw = mk_rsrc(p.wd + cg * 128, F_ * D_ * 2 - cg * 128);
    w.ldsb = ldsb;
    const unsigned doff = (lane >> 3) * 1024 + (((lane & 7) ^ (2 * ((lane >> 4) & 3))) << 4);
#pragma unroll
    for (int i = 0; i < 4; ++i) w.vd[i] = doff + i * (8192 - 1024);
}
DI void g2w_stage(const G2W& w, int aw, int s) {
    const unsigned m0 = w.ldsb + aw * G2_RING + (s % G2_NS) * 4096, so = (unsigned)(14 * aw + s) * 32768;
    dma_m0<0>(m0, w.vd[0], w.rw, so); dma_c<1024>(w.vd[1], w.rw, so); dma_c<2048>(w.vd[2], w.rw, so); dma_c<3072>(w.vd[3], w.rw, so);
}

struct ConvIn { u32x4 h0[2], h1[2]; unsigned dch; };
DI void conv_load_filter(const Params& p, int cp, int tid, ConvIn& in) {
#pragma unroll
    for (int c2 = 0; c2 < 2; ++c2) {
        const rsrc_t r = mk_rsrc(p.ssm_k + (size_t)(2 * cp + c2) * 4096, 4096);
        in.h0[c2] = __builtin_amdgcn_raw_buffer_load_b128(r, tid * 16, 0, 0);
        in.h1[c2] = __builtin_amdgcn_raw_buffer_load_b128(r, tid * 16 + 16, 0, 0);
    }
    in.dch = __builtin_amdgcn_raw_buffer_load_b16(mk_rsrc(p.ssm_D, D_ * 2), (2 * cp + (tid >> 7)) * 2, 0, 0);
}
DI void conv_build_filter(lchar* lds, int tid, const ConvIn& in) {
#pragma unroll
    for (int c2 = 0; c2 < 2; ++c2) {
        lchar* base = lds + L_CV + c2 * CV_CH;
        auto emit = [&](int chunk, u32x4 a, u32x4 b) {
            u32x4 o;
            o = (u32x4){__builtin_amdgcn_alignbit(a.y, a.x, 16), __builtin_amdgcn_alignbit(a.z, a.y, 16), __builtin_amdgcn_alignbit(a.w, a.z, 16), __builtin_amdgcn_alignbit(b.x, a.w, 16)};
            *(LAS u32x4*)(base + 0 * CV_CP + chunk * 16) = o;
            o = (u32x4){a.y, a.z, a.w, b.x};
            *(LAS u32x4*)(base + 1 * CV_CP + chunk * 16) = o;
            o = (u32x4){__builtin_amdgcn_alignbit(a.z, a.y, 16), __builtin_amdgcn_alignbit(a.w, a.z, 16), __builtin_amdgcn_alignbit(b.x, a.w, 16), __builtin_amdgcn_alignbit(b.y, b.x, 16)};
            *(LAS u32x4*)(base + 2 * CV_CP + chunk * 16) = o;
            o = (u32x4){a.z, a.w, b.x, b.y};
            *(LAS u32x4*)(base + 3 * CV_CP + chunk * 16) = o;
        };
        emit(tid + 2, in.h0[c2], in.h1[c2]);
        if (tid < 2) { const u32x4 z = {0, 0, 0, 0}; emit(1 - tid, z, tid == 0 ? in.h0[c2] : z); }
        if (tid < 32) *(LAS u32x4*)(base + CV_X + tid * 16) = (u32x4){0, 0, 0, 0};
    }
}
DI void ldv_dev_asm(u32x4& d, unsigned voff, rsrc_t r, unsigned soff) {
    asm volatile("buffer_load_dwordx4 %0, %1, %2, %3 offen sc1" : "=v"(d) : "v"(voff), "s"(r), "s"(soff) : "memory");
}
struct ConvAcc { f32x4 acc[8]; };
DI void conv_main(lchar* lds, int bw, int lane, ConvAcc& A_, const G2W& w2) {
    const int c2 = bw >> 1, half = bw & 1;
    lchar* base = lds + L_CV + c2 * CV_CH;
    const int r = lane & 15, q = lane >> 4;
    lchar* ap = base + (r & 3) * CV_CP + ((r >> 2) * 4 - 8 * q + 24) * 2 + 256 * half;
    lchar* bp = base + CV_X + 512 + (16 * r - 16 + 8 * q) * 2 - 256 * half;
    f32x4* acc = A_.acc;
#pragma unroll
    for (int g = 0; g < 8; ++g) acc[g] = (f32x4){0, 0, 0, 0};
    bf16x4 Al[2][8], Ah[2][8]; bf16x8 B[2][8];
    auto rd = [&](auto S, int ww, int i) {
        constexpr int s = S.value;
        const int u = i / 3, k = i % 3;
        if (k == 0) B[s][u] = lds_b128(bp - 64 * ww + 512 * u);
        else if (k == 1) Al[s][u] = *(LAS bf16x4*)(ap + 64 * ww + 512 * u);
        else Ah[s][u] = *(LAS bf16x4*)(ap + 64 * ww + 512 * u + 8);
    };
    auto step = [&](auto CUR, int wnext) {
        constexpr int cur = CUR.value;
        std::integral_constant<int, cur ^ 1> NXT;
        int g = 0;
#pragma unroll
        for (int sum = 0; sum < 8; ++sum)
#pragma unroll
            for (int u = 0; u <= sum; ++u) {
                const int t = sum - u;
                acc[sum] = mfma(cat8(Al[cur][u], Ah[cur][u]), B[cur][t], acc[sum]);
                SCHED();
                if (wnext >= 0 && g < 24) rd(NXT, wnext, g);
                ++g;
                SCHED();
            }
    };
    std::integral_constant<int, 0> I0; std::integral_constant<int, 1> I1;
#pragma unroll
    for (int i = 0; i < 24; ++i) rd(I0, 0, i);
    SCHED();
    step(I0, 1);
    lds_wait(lds + L_CNTA, 4);
#pragma unroll
    for (int s = 0; s < 3; ++s) g2w_stage(w2, 2 + (bw >> 1), 3 * (bw & 1) + s);
    SCHED();
    step(I1, 2); step(I0, 3);
    WAIT_V(0); lds_arrive(lds + L_RING, lane);
    step(I1, -1);
#pragma unroll
    for (int j = 0; j < 4; ++j) {
        f32x4 a = acc[j], b = acc[4 + j];
        asm volatile("" : "+v"(a), "+v"(b));
        acc[j] = half ? b : a; acc[4 + j] = half ? a : b;
        *(LAS f32x4*)(lds + L_EX + bw * 4096 + j * 1024 + lane * 16) = acc[4 + j];
    }
    lds_arrive(lds + L_PAIR + bw * 4, lane);
    lds_wait(lds + L_PAIR + (bw ^ 1) * 4, 1);
}
DI void conv_finish(const Params& p, lchar* lds, int cp, int bw, int lane, unsigned tag, ConvAcc& A_, unsigned dbits) {
    const int c2 = bw >> 1, half = bw & 1;
    lchar* base = lds + L_CV + c2 * CV_CH;
    const int r = lane & 15, q = lane >> 4;
    const int ch = 2 * cp + c2;
    const float dch = bf_lo(dbits);
    lchar* stg = base + half * 2048;
#pragma unroll
    for (int j = 0; j < 4; ++j) {
        const f32x4 y = A_.acc[j] + *(LAS f32x4*)(lds + L_EX + (bw ^ 1) * 4096 + j * 1024 + lane * 16);
        const int t0 = 256 * (4 * half + j) + 16 * r + 4 * q;
        const u32x2 kv = *(LAS u32x2*)(base + CV_X + 512 + ((t0 & ~7) + 4 - (t0 & 7)) * 2);
        u32x2 o;
        o.x = pk_bf16(y[0] + bf_hi(kv.y) * dch, y[1] + bf_lo(kv.y) * dch);
        o.y = pk_bf16(y[2] + bf_hi(kv.x) * dch, y[3] + bf_lo(kv.x) * dch);
        *(LAS u32x2*)(stg + (t0 & 1023) * 2) = o;
    }
    WAIT_L(0);
    const rsrc_t ry = mk_rsrc(p.ws + WS_Y2T, D_ * T_ * 2);
#pragma unroll
    for (int i = 0; i < 2; ++i) {
        const u32x4 o = *(LAS u32x4*)(stg + i * 1024 + lane * 16);
        st16_dev(o, ry, lane * 16, ch * 4096 + half * 2048 + i * 1024);
    }
    publish(p.ws + WS_YFLAG + ((cp >> 5) * 128 + (cp & 31) * 4 + bw) * 4, tag, lane);
}

DI void ldh(bf16x8& d, unsigned voff, rsrc_t r, unsigned soff) {
    asm volatile("buffer_load_dwordx4 %0, %1, %2, %3 offen sc1" : "=v"(d) : "v"(voff), "s"(r), "s"(soff) : "memory");
}
constexpr int g2_nD(int s) { return (s >= 1 && s <= 4) ? 4 : 0; }
constexpr int g2_nH(int s) { return (s >= 0 && s <= 9) ? 4 : 0; }
template <int S> DI constexpr int g2_wait() {
    int n = 0;
    for (int s = S - 3; s <= S - 1; ++s) if (s >= 0) n += g2_nD(s) + g2_nH(s);
    if (S < 3) n += 4 * (3 - S);
    return n;
}
#define LATE_STEP 5
template <int S> DI void g2_step(lchar* lds, int lane, lchar* ring, const int (&ar)[4], const G2W& w, int aw, rsrc_t rh, unsigned hv, unsigned hs, bf16x8 (&H)[5][4], bf16x4 (&Wf)[4][4][2], f32x4 (&acc)[4][4]) {
    constexpr int cur = S & 3, hc = S % 5;
    asm volatile("s_waitcnt vmcnt(%4)" : "+v"(H[hc][0]), "+v"(H[hc][1]), "+v"(H[hc][2]), "+v"(H[hc][3]) : "i"(g2_wait<S>()) : "memory");
    SCHED();
    if (S == LATE_STEP && aw == 0 && lane == 0) *(volatile LAS unsigned*)(lds + L_LATE) = 1;
    const unsigned m0 = w.ldsb + aw * G2_RING + ((S + 9) % G2_NS) * 4096, so = (unsigned)(14 * aw + S + 9) * 32768;
#pragma unroll
    for (int n = 0; n < 4; ++n) {
        const bf16x8 wf = cat8(Wf[cur][n][0], Wf[cur][n][1]);
#pragma unroll
        for (int m = 0; m < 4; ++m) {
            acc[n][m] = mfma(wf, H[hc][m], acc[n][m]);
            SCHED();
            const int g = 4 * n + m;
            if (m == 3 && S + 4 <= 13) {
                Wf[cur][n][0] = lds_tr(ring + ((S + 4) % G2_NS) * 4096 + ar[n]);
                Wf[cur][n][1] = lds_tr(ring + ((S + 4) % G2_NS) * 4096 + ar[n] + 2048);
            }
            if (g2_nD(S)) {
                if (g == 3) { WAIT_L(2); dma_m0<0>(m0, w.vd[0], w.rw, so); }
                if (g == 5) dma_c<1024>(w.vd[1], w.rw, so);
                if (g == 7) dma_c<2048>(w.vd[2], w.rw, so);
                if (g == 9) dma_c<3072>(w.vd[3], w.rw, so);
                if (g2_nH(S) && g >= 10 && g <= 13) ldh(H[(S + 4) % 5][g - 10], hv, rh, hs + ((S + 4) * 4 + (g - 10)) * 1024);
            } else if (g2_nH(S) && (g & 3) == 1) ldh(H[(S + 4) % 5][g >> 2], hv, rh, hs + ((S + 4) * 4 + (g >> 2)) * 1024);
            SCHED();
        }
    }
}
template <int... S> DI void g2_steps(std::integer_sequence<int, S...>, lchar* lds, int lane, lchar* ring, const int (&ar)[4], const G2W& w, int aw, rsrc_t rh, unsigned hv, unsigned hs, bf16x8 (&H)[5][4], bf16x4 (&Wf)[4][4][2], f32x4 (&acc)[4][4]) {
    (g2_step<S>(lds, lane, ring, ar, w, aw, rh, hv, hs, H, Wf, acc), ...);
}
DI void gemm2_main(const Params& p, lchar* lds, unsigned ldsb, int tb, int cg, int aw, int lane, unsigned tag STAMP_ARG) {
    const int wid = aw; (void)wid;
    const int c = lane & 15, q = lane >> 4, qq = (lane & 15) >> 2, pp = lane & 3;
    G2W w; g2w_init(w, p, ldsb, cg, lane);
    lchar* ring = lds + aw * G2_RING;
    const int sw = 2 * (q & 1) + (qq >> 1);
    int ar[4];
#pragma unroll
    for (int n = 0; n < 4; ++n) ar[n] = (4 * q + qq) * 128 + ((n ^ sw) << 5) + ((pp >> 1) << 4) + ((pp & 1) << 3);
    f32x4 acc[4][4];
#pragma unroll
    for (int n = 0; n < 4; ++n)
#pragma unroll
        for (int m = 0; m < 4; ++m) acc[n][m] = (f32x4){0, 0, 0, 0};
    bf16x8 H[5][4]; bf16x4 Wf[4][4][2];
    const rsrc_t rh = mk_rsrc(p.ws + WS_H, T_ * F_ * 2);
    const unsigned hv = lane * 16, hs = (unsigned)(tb * 56 + 14 * aw) * 4096;
    lds_wait(lds + L_RING, 4);
#pragma unroll
    for (int s = 0; s < 4; ++s)
#pragma unroll
        for (int g = 0; g < 8; ++g) Wf[s][g >> 1][g & 1] = lds_tr(ring + s * 4096 + ar[g >> 1] + (g & 1) * 2048);
#pragma unroll
    for (int s = 0; s < 4; ++s)
        asm volatile("s_waitcnt lgkmcnt(0)" : "+v"(Wf[s][0][0]), "+v"(Wf[s][0][1]), "+v"(Wf[s][1][0]), "+v"(Wf[s][1][1]), "+v"(Wf[s][2][0]), "+v"(Wf[s][2][1]), "+v"(Wf[s][3][0]), "+v"(Wf[s][3][1]) :: "memory");
#pragma unroll
    for (int s = 6; s < 10; ++s) g2w_stage(w, aw, s);
    STAMP(3);
    if (aw == 0) {
        poll<1>(p.ws + WS_HFLAG + (tb >> 1) * 256, 56, tag, lane);
        lds_arrive(lds + L_HRDY, lane);
    } else lds_wait(lds + L_HRDY, 1);
    STAMP(8); STAMPC(12);
#pragma unroll
    for (int s = 0; s < 4; ++s)
#pragma unroll
        for (int m = 0; m < 4; ++m) ldh(H[s][m], hv, rh, hs + (s * 4 + m) * 1024);
    SCHED();
    g2_steps(std::make_integer_sequence<int, 14>{}, lds, lane, ring, ar, w, aw, rh, hv, hs, H, Wf, acc);
    STAMP(9); STAMPC(13);
    WAIT_L(0); BAR();
#pragma unroll
    for (int m = 0; m < 4; ++m)
#pragma unroll
        for (int n = 0; n < 4; ++n)
            *(LAS f32x4*)(lds + aw * 16384 + (16 * m + c) * 256 + (((4 * n + q) ^ c) << 4)) = acc[n][m];
}
struct SideIn { u32x4 qv[2]; };
DI void side_early(const Params& p, lchar* lds, int tb, int cg, int bw, int lane, unsigned tag, SideIn& in) {
#pragma unroll
    for (int i = 0; i < 2; ++i) in.qv[i] = gload16(p.q + (size_t)(64 * tb + 16 * bw + 8 * i + (lane >> 3)) * 1024 + cg * 128 + (lane & 7) * 16);
}
DI void side_late(const Params& p, lchar* lds, int tb, int cg, int bw, int lane, unsigned tag, const SideIn& in) {
    if (bw == 3) {
        lds_wait<8>(lds + L_LATE, 1);
        poll<1, 16>(p.ws + WS_GFLAG + tb * 32, 8, tag, lane);
        const unsigned g = ld4_dev(p.ws + WS_GATE + (64 * tb + lane) * 4);
        *(LAS unsigned*)(lds + L_SCR + lane * 4) = g;
    }
    if (bw == 0) { lds_wait<8>(lds + L_LATE, 1); poll<2, 16>(p.ws + WS_YFLAG + cg * 512, 128, tag, lane); lds_arrive(lds + L_YRDY, lane); }
    else lds_wait<8>(lds + L_YRDY, 1);
    const rsrc_t ry = mk_rsrc(p.ws + WS_Y2T, D_ * T_ * 2);
    u32x4 yv[2];
#pragma unroll
    for (int i = 0; i < 2; ++i) yv[i] = ld16_dev(ry, (64 * cg + 16 * bw + 8 * i + (lane >> 3)) * 4096 + (lane & 7) * 16, tb * 128);
    lchar* slot = lds + L_EX + (bw ^ 1) * 4096;
#pragma unroll
    for (int i = 0; i < 2; ++i) {
        const int row = 8 * i + (lane >> 3);
        *(LAS u32x4*)(slot + row * 128 + (((lane & 7) ^ (row & 7)) << 4)) = in.qv[i];
        *(LAS u32x4*)(slot + 2048 + row * 128 + (lane & 7) * 16) = yv[i];
    }
}
DI void final_mix(const Params& p, lchar* lds, int tb, int cg, int wid, int lane) {
    const int c = lane & 15, q = lane >> 4, qq = (lane & 15) >> 2, pp = lane & 3;
    const int tg = wid >> 1, ps = wid & 1;
    const int tok = 16 * tg + c, d0 = 32 * ps + 8 * q;
    const float gate = *(LAS float*)(lds + L_SCR + tok * 4);
    f32x4 b0 = {0, 0, 0, 0}, b1 = {0, 0, 0, 0};
#pragma unroll
    for (int w2 = 0; w2 < 4; ++w2) {
        b0 += *(LAS f32x4*)(lds + w2 * 16384 + tok * 256 + ((((d0 >> 2)) ^ c) << 4));
        b1 += *(LAS f32x4*)(lds + w2 * 16384 + tok * 256 + ((((d0 >> 2) + 1) ^ c) << 4));
    }
    lchar* ya = lds + L_EX + ((d0 >> 4) ^ 1) * 4096 + 2048 + ((d0 & 15) + qq) * 128 + (16 * tg + 4 * pp) * 2;
    const u32x4 yv = __builtin_bit_cast(u32x4, cat8(lds_tr(ya), lds_tr(ya + 512)));
    const u32x4 qv = *(LAS u32x4*)(lds + L_EX + (tg ^ 1) * 4096 + c * 128 + (((d0 >> 3) ^ (c & 7)) << 4));
    const float g1 = gate, g0 = 1.0f - gate;
    u32x4 o;
    o.x = pk_bf16(g1 * bf_lo(qv.x) * bf_lo(yv.x) + g0 * b0[0], g1 * bf_hi(qv.x) * bf_hi(yv.x) + g0 * b0[1]);
    o.y = pk_bf16(g1 * bf_lo(qv.y) * bf_lo(yv.y) + g0 * b0[2], g1 * bf_hi(qv.y) * bf_hi(yv.y) + g0 * b0[3]);
    o.z = pk_bf16(g1 * bf_lo(qv.z) * bf_lo(yv.z) + g0 * b1[0], g1 * bf_hi(qv.z) * bf_hi(yv.z) + g0 * b1[1]);
    o.w = pk_bf16(g1 * bf_lo(qv.w) * bf_lo(yv.w) + g0 * b1[2], g1 * bf_hi(qv.w) * bf_hi(yv.w) + g0 * b1[3]);
    lchar* stg = lds + L_OST + wid * 1024;
    *(LAS u32x4*)(stg + c * 64 + ((q ^ ((c >> 1) & 3)) << 4)) = o;
    WAIT_L(0);
    const int r2 = lane >> 2, k2 = lane & 3;
    const u32x4 o2 = *(LAS u32x4*)(stg + r2 * 64 + ((k2 ^ ((r2 >> 1) & 3)) << 4));
    *(GAS u32x4*)(p.out + (size_t)(64 * tb + 16 * tg + r2) * 1024 + cg * 128 + ps * 64 + k2 * 16) = o2;
}

__global__ void __launch_bounds__(512, 2) fused_kernel(Params p) {
    extern __shared__ __attribute__((aligned(16))) char smem[];
    lchar* lds = (lchar*)smem;
    const unsigned ldsb = (unsigned)(size_t)lds;
    const int wid = rfl(threadIdx.x >> 6), lane = threadIdx.x & 63;
    const int xcd = blockIdx.x & 7, slot = blockIdx.x >> 3;
    const bool helper = slot >= 28;
    const int fq = xcd >> 1, mh = xcd & 1;
    const int fb = (fq >> 1) * 7 + (fq & 1) * 4;
    const int ft = slot < 24 ? fb + (slot >> 3) : (fq >> 1) * 7 + 3;
    const int mt = slot < 24 ? 8 * mh + (slot & 7) : 8 * mh + 4 * (fq & 1) + (slot - 24);
    const int tb = 16 * mh + 4 * fq + (slot >> 3), cg = slot & 7;
    const int cp = blockIdx.x;
    unsigned tag;
    STAMP_DECL
    STAMP(0);
    if (wid == 0 && lane > 0 && lane < 16) *(LAS unsigned*)(lds + L_TAG + lane * 4) = 0;
    if (__builtin_expect(helper, 0)) { WAIT_L(0); BAR(); }
    if (__builtin_expect(wid >= 4, 1)) {
        const int bw = wid - 4, tid = bw * 64 + lane;
        ConvIn cin;
        u32x4 xv[2];
        const rsrc_t rkv = mk_rsrc(p.ws + WS_KVT, D_ * T_ * 2);
        bool kv_ok;
        if (__builtin_expect(!helper, 1)) {
            G1Load L; L.init(p, ldsb, mt, ft, bw, lane);
            asm volatile("s_nop 4");
            L.stage(0, 0); L.stage(1, 1);
#pragma unroll
            for (int kt = 0; kt < 8; ++kt) {
                if (kt <= 5) WAIT_V(12); else if (kt == 6) WAIT_V(17); else WAIT_V(2);
                BAR();
                if (kt == 0) STAMP(1);
                if (kt + 2 < 8) L.stage(kt + 2, (kt + 2) % 3);
                if (kt == 5) { conv_load_filter(p, cp, tid, cin); FENCE(); }
                if (kt == 6) {
                    tag = rfl(*(LAS unsigned*)(lds + L_TAG));
                    const unsigned f = ld4_dev(p.ws + WS_KVFLAG + (cp >> 5) * 128 + (lane & 31) * 4);
                    conv_build_filter(lds, tid, cin);
                    FENCE();
                    kv_ok = __all(f == tag);
                    if (kv_ok) { xv[0] = ld16_dev(rkv, tid * 16, (2 * cp) * 4096); xv[1] = ld16_dev(rkv, tid * 16, (2 * cp + 1) * 4096); }
                    FENCE();
                }
            }
        } else {
            tag = prep_phase(p, lds, xcd * 4 + (slot - 28), wid, lane STAMP_PASS);
            lds_arrive(lds + L_CNTP, lane);
            conv_load_filter(p, cp, tid, cin);
            conv_build_filter(lds, tid, cin);
            kv_ok = false;
        }
        STAMP(2);
        if (!kv_ok) {
            poll<1, 2>(p.ws + WS_KVFLAG + (cp >> 5) * 128, 32, tag, lane);
            xv[0] = ld16_dev(rkv, tid * 16, (2 * cp) * 4096); xv[1] = ld16_dev(rkv, tid * 16, (2 * cp + 1) * 4096);
        }
#pragma unroll
        for (int c2 = 0; c2 < 2; ++c2) *(LAS u32x4*)(lds + L_CV + c2 * CV_CH + CV_X + 512 + tid * 16) = xv[c2];
        lds_arrive(lds + L_CNTB, lane);
        if (helper) lds_wait(lds + L_CNTP, 8);
        G2W w2; g2w_init(w2, p, ldsb, cg, lane);
#pragma unroll
        for (int s = 0; s < 3; ++s) g2w_stage(w2, bw >> 1, 3 * (bw & 1) + s);
        SideIn si;
        side_early(p, lds, tb, cg, bw, lane, tag, si);
        lds_wait(lds + L_CNTB, 4);
        STAMP(3);
        ConvAcc ca;
        conv_main(lds, bw, lane, ca, w2);
        STAMP(4);
        conv_finish(p, lds, cp, bw, lane, tag, ca, cin.dch);
        STAMP(5);
        side_late(p, lds, tb, cg, bw, lane, tag, si);
        if (blockIdx.x == 0 && bw == 0) {
            for (int j = 0; j < 8; ++j) poll<2, 16>(p.ws + WS_YFLAG + j * 512, 128, tag, lane);
            if (lane == 0) st4_dev(p.ws + WS_EPOCH, tag);
        }
        STAMP(6);
        WAIT_L(0); BAR();
    } else {
        if (!helper) {
            if (wid == 0) {
                const unsigned t = mk_tag(ld4_dev(p.ws + WS_EPOCH));
                if (lane == 0) *(LAS unsigned*)(lds + L_TAG) = t;
                WAIT_L(0);
            }
            G1Comp G;
            G.kloop(lds, wid, lane);
            STAMP(2);
            lds_arrive(lds + L_CNTA, lane);
            tag = rfl(*(LAS unsigned*)(lds + L_TAG));
            G.epilogue(p, mt, ft, wid, lane);
            STAMP(4);
            publish(p.ws + WS_HFLAG + (mt * 64 + ft * 4 + wid) * 4, tag, lane);
            STAMP(5);
        } else {
            tag = prep_phase(p, lds, xcd * 4 + (slot - 28), wid, lane STAMP_PASS);
            lds_arrive(lds + L_CNTP, lane);
            lds_arrive(lds + L_CNTA, lane);
            STAMP(2);
        }
        gemm2_main(p, lds, ldsb, tb, cg, wid, lane, tag STAMP_PASS);
        STAMP(6);
    }
    WAIT_L(0); BAR();
    final_mix(p, lds, tb, cg, wid, lane);
    STAMP(7);
    STAMP_END;
}

extern "C" void kernel_launch(void* const* d_in, const int* in_sizes, int n_in, void* d_out, int out_size, void* d_ws, size_t ws_size, hipStream_t stream) {
    Params p;
    p.q = (const char*)d_in[0]; p.k = (const char*)d_in[1]; p.v = (const char*)d_in[2]; p.shift_k = (const char*)d_in[3];
    p.ssm_k = (const char*)d_in[4]; p.ssm_D = (const char*)d_in[5];
    p.wg = (const char*)d_in[7]; p.wu = (const char*)d_in[8]; p.wd = (const char*)d_in[9]; p.gate_w = (const char*)d_in[10];
    p.out = (char*)d_out; p.ws = (char*)d_ws;
    (void)hipFuncSetAttribute((const void*)fused_kernel, hipFuncAttributeMaxDynamicSharedMemorySize, LDS_BYTES);
    fused_kernel<<<256, 512, LDS_BYTES, stream>>>(p);
}
```

```cpp
#include <hip/hip_runtime.h>
#include <hip/hip_bf16.h>
#include <stdint.h>
#include <type_traits>

#define LAS __attribute__((address_space(3)))
#define GAS __attribute__((address_space(1)))
#define DI __device__ __forceinline__
typedef __bf16 bf16x8 __attribute__((ext_vector_type(8)));
typedef __bf16 bf16x4 __attribute__((ext_vector_type(4)));
typedef __bf16 bf16x2 __attribute__((ext_vector_type(2)));
typedef float f32x4 __attribute__((ext_vector_type(4)));
typedef float f32x2 __attribute__((ext_vector_type(2)));
typedef unsigned u32x4 __attribute__((ext_vector_type(4)));
typedef unsigned u32x2 __attribute__((ext_vector_type(2)));
typedef LAS char lchar;
typedef __amdgpu_buffer_rsrc_t rsrc_t;
typedef GAS unsigned gu32;

constexpr int T_ = 2048, D_ = 512, F_ = 1792;
constexpr unsigned WS_EPOCH = 0;
constexpr unsigned WS_HFLAG = 1024;
constexpr unsigned WS_KVFLAG = 8192;
constexpr unsigned WS_YFLAG = 12288;
constexpr unsigned WS_GFLAG = 20480;
constexpr unsigned WS_STAMP = 32768;
constexpr unsigned WS_GATE = 65536;
constexpr unsigned WS_KVT = 131072;
constexpr unsigned WS_Y2T = WS_KVT + D_ * T_ * 2;
constexpr unsigned WS_H = WS_Y2T + D_ * T_ * 2;
constexpr unsigned WS_END = WS_H + T_ * F_ * 2;
constexpr int G1_STAGE = 49152;
constexpr int G2_NS = 6, G2_RING = G2_NS * 4096;
constexpr int L_OST = 65536;
constexpr int CV_CP = 4160, CV_X = 4 * CV_CP, CV_CH = CV_X + 4608;
constexpr int L_CV = 98304;
constexpr int L_EX = L_CV + 2 * CV_CH;
constexpr int L_SCR = L_EX + 16384;
constexpr int L_TAG = L_SCR + 256;
constexpr int L_CNTA = L_TAG + 4, L_CNTB = L_TAG + 8, L_PAIR = L_TAG + 12, L_HRDY = L_TAG + 28, L_KVRDY = L_TAG + 32, L_YRDY = L_TAG + 36, L_RING = L_TAG + 40, L_LATE = L_TAG + 44, L_CNTP = L_TAG + 48;
constexpr int L_PSK = L_TAG + 64;
constexpr int LDS_BYTES = L_PSK + 4096;
static_assert(LDS_BYTES <= 163840, "lds");

struct Params {
    const char* q; const char* k; const char* v; const char* shift_k; const char* ssm_k; const char* ssm_D;
    const char* wg; const char* wu; const char* wd; const char* gate_w;
    char* out; char* ws;
};

#define WAIT_V(n) asm volatile("s_waitcnt vmcnt(" #n ")" ::: "memory")
#define WAIT_VN(n) asm volatile("s_waitcnt vmcnt(%0)" :: "i"(n) : "memory")
#define WAIT_L(n) asm volatile("s_waitcnt lgkmcnt(" #n ")" ::: "memory")
#define BAR() do { asm volatile("" ::: "memory"); __builtin_amdgcn_s_barrier(); asm volatile("" ::: "memory"); } while (0)
#define SCHED() __builtin_amdgcn_sched_barrier(0)
#define FENCE() asm volatile("" ::: "memory")

DI rsrc_t mk_rsrc(const void* p, unsigned bytes) { return __builtin_amdgcn_make_buffer_rsrc((void*)p, (short)0, (int)bytes, 0x00020000); }
template <int OFF> DI void dma_m0(unsigned m0, unsigned voff, rsrc_t r, unsigned soff) {
    asm volatile("s_mov_b32 m0, %0\n\ts_nop 0\n\tbuffer_load_dwordx4 %1, %2, %3 offen offset:%4 lds" :: "s"(m0), "v"(voff), "s"(r), "s"(soff), "i"(OFF) : "memory");
}
template <int OFF> DI void dma_c(unsigned voff, rsrc_t r, unsigned soff) {
    asm volatile("buffer_load_dwordx4 %0, %1, %2 offen offset:%3 lds" :: "v"(voff), "s"(r), "s"(soff), "i"(OFF) : "memory");
}
DI void touch(unsigned m0, unsigned voff, rsrc_t r, unsigned soff) {
    asm volatile("s_mov_b32 m0, %0\n\ts_nop 0\n\tbuffer_load_dword %1, %2, %3 offen lds" :: "s"(m0), "v"(voff), "s"(r), "s"(soff) : "memory");
}
DI bf16x8 lds_b128(lchar* p) { return *(LAS bf16x8*)p; }
DI bf16x4 lds_tr(lchar* p) { return __builtin_amdgcn_ds_read_tr16_b64_v4bf16((LAS bf16x4*)p); }
DI bf16x8 cat8(bf16x4 a, bf16x4 b) { return __builtin_shufflevector(a, b, 0, 1, 2, 3, 4, 5, 6, 7); }
DI f32x4 mfma(bf16x8 a, bf16x8 b, f32x4 c) { return __builtin_amdgcn_mfma_f32_16x16x32_bf16(a, b, c, 0, 0, 0); }
DI unsigned pk_bf16(float lo, float hi) { bf16x2 r; r[0] = (__bf16)lo; r[1] = (__bf16)hi; return __builtin_bit_cast(unsigned, r); }
DI float bf_lo(unsigned u) { return __builtin_bit_cast(float, u << 16); }
DI float bf_hi(unsigned u) { return __builtin_bit_cast(float, u & 0xffff0000u); }
DI u32x4 gload16(const char* p) { return *(const GAS u32x4*)p; }
DI unsigned rfl(unsigned x) { return __builtin_amdgcn_readfirstlane(x); }
DI u32x4 ld16_dev(rsrc_t r, unsigned voff, unsigned soff) { return __builtin_amdgcn_raw_buffer_load_b128(r, voff, soff, 16); }
DI void st16_dev(u32x4 v, rsrc_t r, unsigned voff, unsigned soff) { __builtin_amdgcn_raw_buffer_store_b128(v, r, voff + soff, 0, 16); }
DI unsigned ld4_dev(const char* p) { return __hip_atomic_load((gu32*)p, __ATOMIC_RELAXED, __HIP_MEMORY_SCOPE_AGENT); }
DI void st4_dev(char* p, unsigned v) { __hip_atomic_store((gu32*)p, v, __ATOMIC_RELAXED, __HIP_MEMORY_SCOPE_AGENT); }
DI void publish(char* flag, unsigned tag, int lane) { WAIT_V(0); if (lane == 0) st4_dev(flag, tag); }
template <int NV, int SLEEP = 4> DI void poll(const char* flags, int n, unsigned tag, int lane) {
    for (int it = 0; it < 200000; ++it) {
        bool ok = true;
#pragma unroll
        for (int j = 0; j < NV; ++j) { const int i = lane + 64 * j; if (i < n) ok &= (ld4_dev(flags + i * 4) == tag); }
        if (__all(ok)) break;
        __builtin_amdgcn_s_sleep(SLEEP);
    }
    FENCE();
}
DI void lds_arrive(lchar* w, int lane) { WAIT_L(0); if (lane == 0) __hip_atomic_fetch_add((LAS unsigned*)w, 1u, __ATOMIC_RELAXED, __HIP_MEMORY_SCOPE_WORKGROUP); }
template <int SLEEP = 1> DI void lds_wait(lchar* w, unsigned n) {
    for (int it = 0; it < 4000000; ++it) {
        if (rfl(__hip_atomic_load((LAS unsigned*)w, __ATOMIC_RELAXED, __HIP_MEMORY_SCOPE_WORKGROUP)) >= n) break;
        __builtin_amdgcn_s_sleep(SLEEP);
    }
    FENCE();
}
DI unsigned mk_tag(unsigned epoch) { return rfl(epoch) * 1664525u + 1013904223u; }
#define STAMP(i) do {} while (0)
#define STAMP_DECL
#define STAMP_ARG
#define STAMP_PASS
#define STAMP_END do {} while (0)
#define STAMPC(i) do {} while (0)

template <int CTRL> DI float dpp_add(float v) {
    return v + __builtin_bit_cast(float, __builtin_amdgcn_update_dpp(0, __builtin_bit_cast(int, v), CTRL, 0xf, 0xf, true));
}
DI f32x2 geglu2(f32x2 g, f32x2 u) {
    const float k0 = -2.302208198f, k1 = -0.1029432397f;
    f32x2 a = g * ((g * g) * k1 + k0);
    f32x2 e; e.x = __builtin_amdgcn_exp2f(a.x); e.y = __builtin_amdgcn_exp2f(a.y);
    e = e + 1.0f;
    f32x2 r; r.x = __builtin_amdgcn_rcpf(e.x); r.y = __builtin_amdgcn_rcpf(e.y);
    return (g * u) * r;
}
DI unsigned geglu_pk(f32x4 g, f32x4 u, int i) {
    f32x2 h = geglu2((f32x2){g[i], g[i + 1]}, (f32x2){u[i], u[i + 1]});
    return pk_bf16(h.x, h.y);
}

struct G1Load {
    unsigned vx[4], vw[4]; rsrc_t rx, rg, ru; unsigned ldsb; int lw, rot;
    DI void init(const Params& p, unsigned ldsb_, int mt, int ft, int lw_, int lane) {
        ldsb = ldsb_; lw = lw_;
        rot = ((mt + ft) & 1) * 4;
        rx = mk_rsrc(p.q + (size_t)128 * mt * 1024, 128 * 1024);
        rg = mk_rsrc(p.wg + ft * 256, D_ * F_ * 2 - ft * 256);
        ru = mk_rsrc(p.wu + ft * 256, D_ * F_ * 2 - ft * 256);
        const unsigned xoff = (lane >> 3) * 1024 + (((lane & 7) ^ (lane >> 3)) << 4) + lw * 4 * 8192;
        const unsigned woff = (lane >> 4) * (F_ * 2) + (((lane & 15) ^ (2 * (lane >> 4))) << 4) + lw * 16 * (F_ * 2);
#pragma unroll
        for (int i = 0; i < 4; ++i) { vx[i] = xoff + i * (8192 - 1024); vw[i] = (woff ^ ((i & 2) ? 128 : 0)) + i * (4 * F_ * 2 - 1024); }
    }
    DI void piece4(unsigned m0, const unsigned* v, rsrc_t r, unsigned so) {
        dma_m0<0>(m0, v[0], r, so); dma_c<1024>(v[1], r, so); dma_c<2048>(v[2], r, so); dma_c<3072>(v[3], r, so);
    }
    DI void stage(int kt0, int buf) {
        const int kt = (kt0 + rot) & 7;
        const unsigned b = ldsb + buf * G1_STAGE + lw * 4096;
        piece4(b, vx, rx, kt * 128);
        piece4(b + 16384, vw, rg, kt * 64 * F_ * 2);
        piece4(b + 32768, vw, ru, kt * 64 * F_ * 2);
    }
};

struct G1Comp {
    f32x4 ag[4][4], au[4][4];
    DI void kloop(lchar* lds, int wid, int lane) {
        const int wm = wid >> 1, wn = wid & 1;
        const int c = lane & 15, q = lane >> 4, qq = (lane & 15) >> 2, pp = lane & 3;
        const int xr0 = (64 * wm + c) * 128 + ((q ^ (c & 7)) << 4);
        const int sk = qq | ((q & 1) << 2);
        int wr[4];
#pragma unroll
        for (int n = 0; n < 4; ++n) wr[n] = 16384 + (8 * q + qq) * 256 + (((4 * wn + n) ^ sk) << 5) + ((pp >> 1) << 4) + ((pp & 1) << 3);
#pragma unroll
        for (int m = 0; m < 4; ++m)
#pragma unroll
            for (int n = 0; n < 4; ++n) { ag[m][n] = (f32x4){0, 0, 0, 0}; au[m][n] = (f32x4){0, 0, 0, 0}; }
        bf16x8 X[2][4]; bf16x4 Wh[2][8][2];
        auto rdx = [&](lchar* s, int ss, int m) { return lds_b128(s + (xr0 ^ (ss * 64)) + m * 2048); };
        auto rdw = [&](lchar* s, int ss, int j, int hh) { return lds_tr(s + wr[j & 3] + ss * 8192 + (j >> 2) * 16384 + hh * 1024); };
        auto substep = [&](auto CUR, bool rd, lchar* s, int ss) {
            constexpr int cur = CUR.value, nxt = cur ^ 1;
#pragma unroll
            for (int j = 0; j < 8; ++j) {
                const bf16x8 wf = cat8(Wh[cur][j][0], Wh[cur][j][1]);
#pragma unroll
                for (int m = 0; m < 4; ++m) {
                    if (j < 4) ag[m][j] = mfma(wf, X[cur][m], ag[m][j]);
                    else au[m][j - 4] = mfma(wf, X[cur][m], au[m][j - 4]);
                    SCHED();
                    const int g = 4 * j + m;
                    if (rd) {
                        if (g < 2) Wh[nxt][0][g] = rdw(s, ss, 0, g);
                        else if (g < 6) X[nxt][g - 2] = rdx(s, ss, g - 2);
                        else if (g < 20) Wh[nxt][1 + ((g - 6) >> 1)][(g - 6) & 1] = rdw(s, ss, 1 + ((g - 6) >> 1), (g - 6) & 1);
                    }
                    SCHED();
                }
            }
        };
        std::integral_constant<int, 0> I0; std::integral_constant<int, 1> I1;
        BAR();
#pragma unroll
        for (int m = 0; m < 4; ++m) X[0][m] = rdx(lds, 0, m);
#pragma unroll
        for (int j = 0; j < 8; ++j) { Wh[0][j][0] = rdw(lds, 0, j, 0); Wh[0][j][1] = rdw(lds, 0, j, 1); }
        SCHED();
        substep(I0, true, lds, 1);
#pragma unroll
        for (int kt = 1; kt < 8; ++kt) {
            lchar* s = lds + (kt % 3) * G1_STAGE;
            WAIT_L(0); BAR(); SCHED();
            substep(I1, true, s, 0);
            substep(I0, true, s, 1);
        }
        substep(I1, false, lds, 0);
    }
    DI void epilogue(const Params& p, int mt, int ft, int wid, int lane) {
        const int wm = wid >> 1, wn = wid & 1;
        const rsrc_t rh = mk_rsrc(p.ws + WS_H, T_ * F_ * 2);
        const unsigned hb = (unsigned)((2 * mt + wm) * 56 + 4 * ft + 2 * wn) * 4096;
#pragma unroll
        for (int j = 0; j < 2; ++j)
#pragma unroll
            for (int m = 0; m < 4; ++m) {
                u32x4 o;
                o.x = geglu_pk(ag[m][2 * j], au[m][2 * j], 0); o.y = geglu_pk(ag[m][2 * j], au[m][2 * j], 2);
                o.z = geglu_pk(ag[m][2 * j + 1], au[m][2 * j + 1], 0); o.w = geglu_pk(ag[m][2 * j + 1], au[m][2 * j + 1], 2);
                st16_dev(o, rh, lane * 16, hb + (j * 4 + m) * 1024);
            }
    }
};

DI unsigned prep_phase(const Params& p, lchar* lds, int tb, int wid, int lane STAMP_ARG) {
    const int tg = lane >> 3, cc = lane & 7;
    const int t0 = 64 * tb + 8 * tg;
    const int ch0 = 64 * wid + 8 * cc;
    u32x4 kr[11], vr[8], sr[4];
    const rsrc_t rkin = mk_rsrc(p.k, T_ * D_ * 2), rvin = mk_rsrc(p.v, T_ * D_ * 2), rs = mk_rsrc(p.shift_k + 512 * wid, 512);
    const u32x4 sk = __builtin_amdgcn_raw_buffer_load_b128(rs, (lane & 31) * 16, 0, 0);
#pragma unroll
    for (int i = 0; i < 11; ++i) kr[i] = __builtin_amdgcn_raw_buffer_load_b128(rkin, (unsigned)((t0 - 3 + i) * 1024 + ch0 * 2), 0, 0);
#pragma unroll
    for (int i = 0; i < 8; ++i) vr[i] = __builtin_amdgcn_raw_buffer_load_b128(rvin, (unsigned)((t0 + i) * 1024 + ch0 * 2), 0, 0);
    const unsigned epoch = ld4_dev(p.ws + WS_EPOCH);
    lchar* st = lds + wid * 8192;
    {
        lchar* s = lds + L_PSK + wid * 512;
        *(LAS u32x4*)(s + (lane & 31) * 16) = sk;
        WAIT_L(0);
#pragma unroll
        for (int i = 0; i < 4; ++i) sr[i] = *(LAS u32x4*)(s + cc * 64 + i * 16);
    }
#pragma unroll
    for (int j = 0; j < 4; ++j) {
        const f32x2 s0 = {bf_lo(sr[j][0]), bf_lo(sr[j][2])}, s1 = {bf_hi(sr[j][0]), bf_hi(sr[j][2])};
        const f32x2 s2 = {bf_lo(sr[j][1]), bf_lo(sr[j][3])}, s3 = {bf_hi(sr[j][1]), bf_hi(sr[j][3])};
        f32x2 kk[11], kv[8], vv[8];
#pragma unroll
        for (int r = 0; r < 11; ++r) kk[r] = (f32x2){bf_lo(kr[r][j]), bf_hi(kr[r][j])};
        SCHED();
#pragma unroll
        for (int r = 0; r < 8; ++r) kv[r] = kk[r] * s3;
        SCHED();
#pragma unroll
        for (int r = 0; r < 8; ++r) kv[r] = kk[r + 1] * s2 + kv[r];
        SCHED();
#pragma unroll
        for (int r = 0; r < 8; ++r) { kv[r] = kk[r + 2] * s1 + kv[r]; vv[r] = (f32x2){bf_lo(vr[r][j]), bf_hi(vr[r][j])}; }
        SCHED();
#pragma unroll
        for (int r = 0; r < 8; ++r) kv[r] = kk[r + 3] * s0 + kv[r];
        SCHED();
#pragma unroll
        for (int r = 0; r < 8; ++r) kv[r] = kv[r] * vv[r];
        SCHED();
        u32x4 o0, o1;
        o0.x = pk_bf16(kv[7].x, kv[6].x); o0.y = pk_bf16(kv[5].x, kv[4].x); o0.z = pk_bf16(kv[3].x, kv[2].x); o0.w = pk_bf16(kv[1].x, kv[0].x);
        o1.x = pk_bf16(kv[7].y, kv[6].y); o1.y = pk_bf16(kv[5].y, kv[4].y); o1.z = pk_bf16(kv[3].y, kv[2].y); o1.w = pk_bf16(kv[1].y, kv[0].y);
        *(LAS u32x4*)(st + (8 * cc + 2 * j) * 128 + ((tg ^ cc) << 4)) = o0;
        *(LAS u32x4*)(st + (8 * cc + 2 * j + 1) * 128 + ((tg ^ cc) << 4)) = o1;
    }
    WAIT_L(0);
    STAMP(12);
    const rsrc_t rk = mk_rsrc(p.ws + WS_KVT, D_ * T_ * 2);
#pragma unroll
    for (int i = 0; i < 8; ++i) {
        const int cl = 8 * i + (lane >> 3);
        const u32x4 o = *(LAS u32x4*)(st + cl * 128 + (((lane & 7) ^ i) << 4));
        st16_dev(o, rk, (64 * wid + cl) * 4096 + (lane & 7) * 16, tb * 128);
    }
    const unsigned tag = mk_tag(epoch);
    STAMP(13);
    publish(p.ws + WS_KVFLAG + (wid * 32 + tb) * 4, tag, lane);
    STAMP(14);
    const int i16 = lane & 15, tk = lane >> 4;
    u32x4 gw[4], xv[2][4];
    const rsrc_t rq = mk_rsrc(p.q, T_ * D_ * 2), rw = mk_rsrc(p.gate_w, D_ * 2);
#pragma unroll
    for (int j = 0; j < 4; ++j) gw[j] = __builtin_amdgcn_raw_buffer_load_b128(rw, (i16 + 16 * j) * 16, 0, 0);
#pragma unroll
    for (int g4 = 0; g4 < 2; ++g4)
#pragma unroll
        for (int j = 0; j < 4; ++j) xv[g4][j] = __builtin_amdgcn_raw_buffer_load_b128(rq, (64 * tb + 8 * wid + 4 * g4 + tk) * 1024 + (i16 + 16 * j) * 16, 0, 0);
#pragma unroll
    for (int g4 = 0; g4 < 2; ++g4) {
        f32x2 acc = {0.f, 0.f};
#pragma unroll
        for (int j = 0; j < 4; ++j)
#pragma unroll
            for (int e = 0; e < 4; ++e) acc += (f32x2){bf_lo(xv[g4][j][e]), bf_hi(xv[g4][j][e])} * (f32x2){bf_lo(gw[j][e]), bf_hi(gw[j][e])};
        float a = acc.x + acc.y;
        a = dpp_add<0x111>(a); a = dpp_add<0x112>(a); a = dpp_add<0x114>(a); a = dpp_add<0x118>(a);
        const float g = __builtin_amdgcn_rcpf(1.0f + __builtin_amdgcn_exp2f(-1.4426950408889634f * a));
        if (i16 == 15) st4_dev(p.ws + WS_GATE + (64 * tb + 8 * wid + 4 * g4 + tk) * 4, __builtin_bit_cast(unsigned, g));
    }
    publish(p.ws + WS_GFLAG + (tb * 8 + wid) * 4, tag, lane);
    return tag;
}

struct G2W { rsrc_t rw; unsigned vd[4]; unsigned ldsb; };
DI void g2w_init(G2W& w, const Params& p, unsigned ldsb, int cg, int lane) {
    w.rw = mk_rsrc(p.wd + cg * 128, F_ * D_ * 2 - cg * 128);
    w.ldsb = ldsb;
    const unsigned doff = (lane >> 3) * 1024 + (((lane & 7) ^ (2 * ((lane >> 4) & 3))) << 4);
#pragma unroll
    for (int i = 0; i < 4; ++i) w.vd[i] = doff + i * (8192 - 1024);
}
DI void g2w_stage(const G2W& w, int aw, int s) {
    const unsigned m0 = w.ldsb + aw * G2_RING + (s % G2_NS) * 4096, so = (unsigned)(14 * aw + s) * 32768;
    dma_m0<0>(m0, w.vd[0], w.rw, so); dma_c<1024>(w.vd[1], w.rw, so); dma_c<2048>(w.vd[2], w.rw, so); dma_c<3072>(w.vd[3], w.rw, so);
}

struct ConvIn { u32x4 h0[2], h1[2]; unsigned dch; };
DI void conv_load_filter(const Params& p, int cp, int tid, ConvIn& in) {
#pragma unroll
    for (int c2 = 0; c2 < 2; ++c2) {
        const rsrc_t r = mk_rsrc(p.ssm_k + (size_t)(2 * cp + c2) * 4096, 4096);
        in.h0[c2] = __builtin_amdgcn_raw_buffer_load_b128(r, tid * 16, 0, 0);
        in.h1[c2] = __builtin_amdgcn_raw_buffer_load_b128(r, tid * 16 + 16, 0, 0);
    }
    in.dch = __builtin_amdgcn_raw_buffer_load_b16(mk_rsrc(p.ssm_D, D_ * 2), (2 * cp + (tid >> 7)) * 2, 0, 0);
}
DI void conv_build_filter(lchar* lds, int tid, const ConvIn& in) {
#pragma unroll
    for (int c2 = 0; c2 < 2; ++c2) {
        lchar* base = lds + L_CV + c2 * CV_CH;
        auto emit = [&](int chunk, u32x4 a, u32x4 b) {
            u32x4 o;
            o = (u32x4){__builtin_amdgcn_alignbit(a.y, a.x, 16), __builtin_amdgcn_alignbit(a.z, a.y, 16), __builtin_amdgcn_alignbit(a.w, a.z, 16), __builtin_amdgcn_alignbit(b.x, a.w, 16)};
            *(LAS u32x4*)(base + 0 * CV_CP + chunk * 16) = o;
            o = (u32x4){a.y, a.z, a.w, b.x};
            *(LAS u32x4*)(base + 1 * CV_CP + chunk * 16) = o;
            o = (u32x4){__builtin_amdgcn_alignbit(a.z, a.y, 16), __builtin_amdgcn_alignbit(a.w, a.z, 16), __builtin_amdgcn_alignbit(b.x, a.w, 16), __builtin_amdgcn_alignbit(b.y, b.x, 16)};
            *(LAS u32x4*)(base + 2 * CV_CP + chunk * 16) = o;
            o = (u32x4){a.z, a.w, b.x, b.y};
            *(LAS u32x4*)(base + 3 * CV_CP + chunk * 16) = o;
        };
        emit(tid + 2, in.h0[c2], in.h1[c2]);
        if (tid < 2) { const u32x4 z = {0, 0, 0, 0}; emit(1 - tid, z, tid == 0 ? in.h0[c2] : z); }
        if (tid < 32) *(LAS u32x4*)(base + CV_X + tid * 16) = (u32x4){0, 0, 0, 0};
    }
}
DI void ldv_dev_asm(u32x4& d, unsigned voff, rsrc_t r, unsigned soff) {
    asm volatile("buffer_load_dwordx4 %0, %1, %2, %3 offen sc1" : "=v"(d) : "v"(voff), "s"(r), "s"(soff) : "memory");
}
struct ConvAcc { f32x4 acc[8]; };
DI void conv_main(lchar* lds, int bw, int lane, ConvAcc& A_, const G2W& w2) {
    const int c2 = bw >> 1, half = bw & 1;
    lchar* base = lds + L_CV + c2 * CV_CH;
    const int r = lane & 15, q = lane >> 4;
    lchar* ap = base + (r & 3) * CV_CP + ((r >> 2) * 4 - 8 * q + 24) * 2 + 256 * half;
    lchar* bp = base + CV_X + 512 + (16 * r - 16 + 8 * q) * 2 - 256 * half;
    f32x4* acc = A_.acc;
#pragma unroll
    for (int g = 0; g < 8; ++g) acc[g] = (f32x4){0, 0, 0, 0};
    bf16x4 Al[2][8], Ah[2][8]; bf16x8 B[2][8];
    auto rd = [&](auto S, int ww, int i) {
        constexpr int s = S.value;
        const int u = i / 3, k = i % 3;
        if (k == 0) B[s][u] = lds_b128(bp - 64 * ww + 512 * u);
        else if (k == 1) Al[s][u] = *(LAS bf16x4*)(ap + 64 * ww + 512 * u);
        else Ah[s][u] = *(LAS bf16x4*)(ap + 64 * ww + 512 * u + 8);
    };
    auto step = [&](auto CUR, int wnext) {
        constexpr int cur = CUR.value;
        std::integral_constant<int, cur ^ 1> NXT;
        int g = 0;
#pragma unroll
        for (int sum = 0; sum < 8; ++sum)
#pragma unroll
            for (int u = 0; u <= sum; ++u) {
                const int t = sum - u;
                acc[sum] = mfma(cat8(Al[cur][u], Ah[cur][u]), B[cur][t], acc[sum]);
                SCHED();
                if (wnext >= 0 && g < 24) rd(NXT, wnext, g);
                ++g;
                SCHED();
            }
    };
    std::integral_constant<int, 0> I0; std::integral_constant<int, 1> I1;
#pragma unroll
    for (int i = 0; i < 24; ++i) rd(I0, 0, i);
    SCHED();
    step(I0, 1);
    lds_wait(lds + L_CNTA, 4);
#pragma unroll
    for (int s = 0; s < 3; ++s) g2w_stage(w2, 2 + (bw >> 1), 3 * (bw & 1) + s);
    SCHED();
    step(I1, 2); step(I0, 3);
    WAIT_V(0); lds_arrive(lds + L_RING, lane);
    step(I1, -1);
#pragma unroll
    for (int j = 0; j < 4; ++j) {
        f32x4 a = acc[j], b = acc[4 + j];
        asm volatile("" : "+v"(a), "+v"(b));
        acc[j] = half ? b : a; acc[4 + j] = half ? a : b;
        *(LAS f32x4*)(lds + L_EX + bw * 4096 + j * 1024 + lane * 16) = acc[4 + j];
    }
    lds_arrive(lds + L_PAIR + bw * 4, lane);
    lds_wait(lds + L_PAIR + (bw ^ 1) * 4, 1);
}
DI void conv_finish(const Params& p, lchar* lds, int cp, int bw, int lane, unsigned tag, ConvAcc& A_, unsigned dbits) {
    const int c2 = bw >> 1, half = bw & 1;
    lchar* base = lds + L_CV + c2 * CV_CH;
    const int r = lane & 15, q = lane >> 4;
    const int ch = 2 * cp + c2;
    const float dch = bf_lo(dbits);
    lchar* stg = base + half * 2048;
#pragma unroll
    for (int j = 0; j < 4; ++j) {
        const f32x4 y = A_.acc[j] + *(LAS f32x4*)(lds + L_EX + (bw ^ 1) * 4096 + j * 1024 + lane * 16);
        const int t0 = 256 * (4 * half + j) + 16 * r + 4 * q;
        const u32x2 kv = *(LAS u32x2*)(base + CV_X + 512 + ((t0 & ~7) + 4 - (t0 & 7)) * 2);
        u32x2 o;
        o.x = pk_bf16(y[0] + bf_hi(kv.y) * dch, y[1] + bf_lo(kv.y) * dch);
        o.y = pk_bf16(y[2] + bf_hi(kv.x) * dch, y[3] + bf_lo(kv.x) * dch);
        *(LAS u32x2*)(stg + (t0 & 1023) * 2) = o;
    }
    WAIT_L(0);
    const rsrc_t ry = mk_rsrc(p.ws + WS_Y2T, D_ * T_ * 2);
#pragma unroll
    for (int i = 0; i < 2; ++i) {
        const u32x4 o = *(LAS u32x4*)(stg + i * 1024 + lane * 16);
        st16_dev(o, ry, lane * 16, ch * 4096 + half * 2048 + i * 1024);
    }
    publish(p.ws + WS_YFLAG + ((cp >> 5) * 128 + (cp & 31) * 4 + bw) * 4, tag, lane);
}

DI void ldh(bf16x8& d, unsigned voff, rsrc_t r, unsigned soff) {
    asm volatile("buffer_load_dwordx4 %0, %1, %2, %3 offen sc1" : "=v"(d) : "v"(voff), "s"(r), "s"(soff) : "memory");
}
constexpr int g2_nD(int s) { return (s >= 1 && s <= 4) ? 4 : 0; }
constexpr int g2_nH(int s) { return (s >= 0 && s <= 9) ? 4 : 0; }
template <int S> DI constexpr int g2_wait() {
    int n = 0;
    for (int s = S - 3; s <= S - 1; ++s) if (s >= 0) n += g2_nD(s) + g2_nH(s);
    if (S < 3) n += 4 * (3 - S);
    return n;
}
#define LATE_STEP 5
template <int S> DI void g2_step(lchar* lds, int lane, lchar* ring, const int (&ar)[4], const G2W& w, int aw, rsrc_t rh, unsigned hv, unsigned hs, bf16x8 (&H)[5][4], bf16x4 (&Wf)[4][4][2], f32x4 (&acc)[4][4]) {
    constexpr int cur = S & 3, hc = S % 5;
    asm volatile("s_waitcnt vmcnt(%4)" : "+v"(H[hc][0]), "+v"(H[hc][1]), "+v"(H[hc][2]), "+v"(H[hc][3]) : "i"(g2_wait<S>()) : "memory");
    SCHED();
    if (S == LATE_STEP && aw == 0 && lane == 0) *(volatile LAS unsigned*)(lds + L_LATE) = 1;
    const unsigned m0 = w.ldsb + aw * G2_RING + ((S + 9) % G2_NS) * 4096, so = (unsigned)(14 * aw + S + 9) * 32768;
#pragma unroll
    for (int n = 0; n < 4; ++n) {
        const bf16x8 wf = cat8(Wf[cur][n][0], Wf[cur][n][1]);
#pragma unroll
        for (int m = 0; m < 4; ++m) {
            acc[n][m] = mfma(wf, H[hc][m], acc[n][m]);
            SCHED();
            const int g = 4 * n + m;
            if (m == 3 && S + 4 <= 13) {
                Wf[cur][n][0] = lds_tr(ring + ((S + 4) % G2_NS) * 4096 + ar[n]);
                Wf[cur][n][1] = lds_tr(ring + ((S + 4) % G2_NS) * 4096 + ar[n] + 2048);
            }
            if (g2_nD(S)) {
                if (g == 3) { WAIT_L(2); dma_m0<0>(m0, w.vd[0], w.rw, so); }
                if (g == 5) dma_c<1024>(w.vd[1], w.rw, so);
                if (g == 7) dma_c<2048>(w.vd[2], w.rw, so);
                if (g == 9) dma_c<3072>(w.vd[3], w.rw, so);
                if (g2_nH(S) && g >= 10 && g <= 13) ldh(H[(S + 4) % 5][g - 10], hv, rh, hs + ((S + 4) * 4 + (g - 10)) * 1024);
            } else if (g2_nH(S) && (g & 3) == 1) ldh(H[(S + 4) % 5][g >> 2], hv, rh, hs + ((S + 4) * 4 + (g >> 2)) * 1024);
            SCHED();
        }
    }
}
template <int... S> DI void g2_steps(std::integer_sequence<int, S...>, lchar* lds, int lane, lchar* ring, const int (&ar)[4], const G2W& w, int aw, rsrc_t rh, unsigned hv, unsigned hs, bf16x8 (&H)[5][4], bf16x4 (&Wf)[4][4][2], f32x4 (&acc)[4][4]) {
    (g2_step<S>(lds, lane, ring, ar, w, aw, rh, hv, hs, H, Wf, acc), ...);
}
DI void gemm2_main(const Params& p, lchar* lds, unsigned ldsb, int tb, int cg, int aw, int lane, unsigned tag STAMP_ARG) {
    const int wid = aw; (void)wid;
    const int c = lane & 15, q = lane >> 4, qq = (lane & 15) >> 2, pp = lane & 3;
    G2W w; g2w_init(w, p, ldsb, cg, lane);
    lchar* ring = lds + aw * G2_RING;
    const int sw = 2 * (q & 1) + (qq >> 1);
    int ar[4];
#pragma unroll
    for (int n = 0; n < 4; ++n) ar[n] = (4 * q + qq) * 128 + ((n ^ sw) << 5) + ((pp >> 1) << 4) + ((pp & 1) << 3);
    f32x4 acc[4][4];
#pragma unroll
    for (int n = 0; n < 4; ++n)
#pragma unroll
        for (int m = 0; m < 4; ++m) acc[n][m] = (f32x4){0, 0, 0, 0};
    bf16x8 H[5][4]; bf16x4 Wf[4][4][2];
    const rsrc_t rh = mk_rsrc(p.ws + WS_H, T_ * F_ * 2);
    const unsigned hv = lane * 16, hs = (unsigned)(tb * 56 + 14 * aw) * 4096;
    lds_wait(lds + L_RING, 4);
#pragma unroll
    for (int s = 0; s < 4; ++s)
#pragma unroll
        for (int g = 0; g < 8; ++g) Wf[s][g >> 1][g & 1] = lds_tr(ring + s * 4096 + ar[g >> 1] + (g & 1) * 2048);
#pragma unroll
    for (int s = 0; s < 4; ++s)
        asm volatile("s_waitcnt lgkmcnt(0)" : "+v"(Wf[s][0][0]), "+v"(Wf[s][0][1]), "+v"(Wf[s][1][0]), "+v"(Wf[s][1][1]), "+v"(Wf[s][2][0]), "+v"(Wf[s][2][1]), "+v"(Wf[s][3][0]), "+v"(Wf[s][3][1]) :: "memory");
#pragma unroll
    for (int s = 6; s < 10; ++s) g2w_stage(w, aw, s);
    STAMP(3);
    if (aw == 0) {
        poll<1>(p.ws + WS_HFLAG + (tb >> 1) * 256, 56, tag, lane);
        lds_arrive(lds + L_HRDY, lane);
    } else lds_wait(lds + L_HRDY, 1);
    STAMP(8); STAMPC(12);
#pragma unroll
    for (int s = 0; s < 4; ++s)
#pragma unroll
        for (int m = 0; m < 4; ++m) ldh(H[s][m], hv, rh, hs + (s * 4 + m) * 1024);
    SCHED();
    g2_steps(std::make_integer_sequence<int, 14>{}, lds, lane, ring, ar, w, aw, rh, hv, hs, H, Wf, acc);
    STAMP(9); STAMPC(13);
    WAIT_L(0); BAR();
#pragma unroll
    for (int m = 0; m < 4; ++m)
#pragma unroll
        for (int n = 0; n < 4; ++n)
            *(LAS f32x4*)(lds + aw * 16384 + (16 * m + c) * 256 + (((4 * n + q) ^ c) << 4)) = acc[n][m];
}
struct SideIn { u32x4 qv[2]; };
DI void side_early(const Params& p, lchar* lds, int tb, int cg, int bw, int lane, unsigned tag, SideIn& in) {
#pragma unroll
    for (int i = 0; i < 2; ++i) in.qv[i] = gload16(p.q + (size_t)(64 * tb + 16 * bw + 8 * i + (lane >> 3)) * 1024 + cg * 128 + (lane & 7) * 16);
}
DI void side_late(const Params& p, lchar* lds, int tb, int cg, int bw, int lane, unsigned tag, const SideIn& in) {
    if (bw == 3) {
        lds_wait<8>(lds + L_LATE, 1);
        poll<1, 16>(p.ws + WS_GFLAG + tb * 32, 8, tag, lane);
        const unsigned g = ld4_dev(p.ws + WS_GATE + (64 * tb + lane) * 4);
        *(LAS unsigned*)(lds + L_SCR + lane * 4) = g;
    }
    if (bw == 0) { lds_wait<8>(lds + L_LATE, 1); poll<2, 16>(p.ws + WS_YFLAG + cg * 512, 128, tag, lane); lds_arrive(lds + L_YRDY, lane); }
    else lds_wait<8>(lds + L_YRDY, 1);
    const rsrc_t ry = mk_rsrc(p.ws + WS_Y2T, D_ * T_ * 2);
    u32x4 yv[2];
#pragma unroll
    for (int i = 0; i < 2; ++i) yv[i] = ld16_dev(ry, (64 * cg + 16 * bw + 8 * i + (lane >> 3)) * 4096 + (lane & 7) * 16, tb * 128);
    lchar* slot = lds + L_EX + (bw ^ 1) * 4096;
#pragma unroll
    for (int i = 0; i < 2; ++i) {
        const int row = 8 * i + (lane >> 3);
        *(LAS u32x4*)(slot + row * 128 + (((lane & 7) ^ (row & 7)) << 4)) = in.qv[i];
        *(LAS u32x4*)(slot + 2048 + row * 128 + (lane & 7) * 16) = yv[i];
    }
}
DI void final_mix(const Params& p, lchar* lds, int tb, int cg, int wid, int lane) {
    const int c = lane & 15, q = lane >> 4, qq = (lane & 15) >> 2, pp = lane & 3;
    const int tg = wid >> 1, ps = wid & 1;
    const int tok = 16 * tg + c, d0 = 32 * ps + 8 * q;
    const float gate = *(LAS float*)(lds + L_SCR + tok * 4);
    f32x4 b0 = {0, 0, 0, 0}, b1 = {0, 0, 0, 0};
#pragma unroll
    for (int w2 = 0; w2 < 4; ++w2) {
        b0 += *(LAS f32x4*)(lds + w2 * 16384 + tok * 256 + ((((d0 >> 2)) ^ c) << 4));
        b1 += *(LAS f32x4*)(lds + w2 * 16384 + tok * 256 + ((((d0 >> 2) + 1) ^ c) << 4));
    }
    lchar* ya = lds + L_EX + ((d0 >> 4) ^ 1) * 4096 + 2048 + ((d0 & 15) + qq) * 128 + (16 * tg + 4 * pp) * 2;
    const u32x4 yv = __builtin_bit_cast(u32x4, cat8(lds_tr(ya), lds_tr(ya + 512)));
    const u32x4 qv = *(LAS u32x4*)(lds + L_EX + (tg ^ 1) * 4096 + c * 128 + (((d0 >> 3) ^ (c & 7)) << 4));
    const float g1 = gate, g0 = 1.0f - gate;
    u32x4 o;
    o.x = pk_bf16(g1 * bf_lo(qv.x) * bf_lo(yv.x) + g0 * b0[0], g1 * bf_hi(qv.x) * bf_hi(yv.x) + g0 * b0[1]);
    o.y = pk_bf16(g1 * bf_lo(qv.y) * bf_lo(yv.y) + g0 * b0[2], g1 * bf_hi(qv.y) * bf_hi(yv.y) + g0 * b0[3]);
    o.z = pk_bf16(g1 * bf_lo(qv.z) * bf_lo(yv.z) + g0 * b1[0], g1 * bf_hi(qv.z) * bf_hi(yv.z) + g0 * b1[1]);
    o.w = pk_bf16(g1 * bf_lo(qv.w) * bf_lo(yv.w) + g0 * b1[2], g1 * bf_hi(qv.w) * bf_hi(yv.w) + g0 * b1[3]);
    lchar* stg = lds + L_OST + wid * 1024;
    *(LAS u32x4*)(stg + c * 64 + ((q ^ ((c >> 1) & 3)) << 4)) = o;
    WAIT_L(0);
    const int r2 = lane >> 2, k2 = lane & 3;
    const u32x4 o2 = *(LAS u32x4*)(stg + r2 * 64 + ((k2 ^ ((r2 >> 1) & 3)) << 4));
    *(GAS u32x4*)(p.out + (size_t)(64 * tb + 16 * tg + r2) * 1024 + cg * 128 + ps * 64 + k2 * 16) = o2;
}

__global__ void __launch_bounds__(512, 2) fused_kernel(Params p) {
    extern __shared__ __attribute__((aligned(16))) char smem[];
    lchar* lds = (lchar*)smem;
    const unsigned ldsb = (unsigned)(size_t)lds;
    const int wid = rfl(threadIdx.x >> 6), lane = threadIdx.x & 63;
    const int xcd = blockIdx.x & 7, slot = blockIdx.x >> 3;
    const bool helper = slot >= 28;
    const int fq = xcd >> 1, mh = xcd & 1;
    const int fb = (fq >> 1) * 7 + (fq & 1) * 4;
    const int ft = slot < 24 ? fb + (slot >> 3) : (fq >> 1) * 7 + 3;
    const int mt = slot < 24 ? 8 * mh + (slot & 7) : 8 * mh + 4 * (fq & 1) + (slot - 24);
    const int tb = 16 * mh + 4 * fq + (slot >> 3), cg = slot & 7;
    const int cp = blockIdx.x;
    unsigned tag;
    STAMP_DECL
    STAMP(0);
    if (wid == 0 && lane > 0 && lane < 16) *(LAS unsigned*)(lds + L_TAG + lane * 4) = 0;
    if (__builtin_expect(helper, 0)) { WAIT_L(0); BAR(); }
    if (__builtin_expect(wid >= 4, 1)) {
        const int bw = wid - 4, tid = bw * 64 + lane;
        ConvIn cin;
        u32x4 xv[2];
        const rsrc_t rkv = mk_rsrc(p.ws + WS_KVT, D_ * T_ * 2);
        bool kv_ok;
        if (__builtin_expect(!helper, 1)) {
            G1Load L; L.init(p, ldsb, mt, ft, bw, lane);
            asm volatile("s_nop 4");
            L.stage(0, 0); L.stage(1, 1);
#pragma unroll
            for (int kt = 0; kt < 8; ++kt) {
                if (kt <= 5) WAIT_V(12); else if (kt == 6) WAIT_V(17); else WAIT_V(2);
                BAR();
                if (kt == 0) STAMP(1);
                if (kt + 2 < 8) L.stage(kt + 2, (kt + 2) % 3);
                if (kt == 5) { conv_load_filter(p, cp, tid, cin); FENCE(); }
                if (kt == 6) {
                    tag = rfl(*(LAS unsigned*)(lds + L_TAG));
                    const unsigned f = ld4_dev(p.ws + WS_KVFLAG + (cp >> 5) * 128 + (lane & 31) * 4);
                    conv_build_filter(lds, tid, cin);
                    FENCE();
                    kv_ok = __all(f == tag);
                    if (kv_ok) { xv[0] = ld16_dev(rkv, tid * 16, (2 * cp) * 4096); xv[1] = ld16_dev(rkv, tid * 16, (2 * cp + 1) * 4096); }
                    FENCE();
                }
            }
        } else {
            tag = prep_phase(p, lds, xcd * 4 + (slot - 28), wid, lane STAMP_PASS);
            lds_arrive(lds + L_CNTP, lane);
            conv_load_filter(p, cp, tid, cin);
            conv_build_filter(lds, tid, cin);
            kv_ok = false;
        }
        STAMP(2);
        if (!kv_ok) {
            poll<1, 2>(p.ws + WS_KVFLAG + (cp >> 5) * 128, 32, tag, lane);
            xv[0] = ld16_dev(rkv, tid * 16, (2 * cp) * 4096); xv[1] = ld16_dev(rkv, tid * 16, (2 * cp + 1) * 4096);
        }
#pragma unroll
        for (int c2 = 0; c2 < 2; ++c2) *(LAS u32x4*)(lds + L_CV + c2 * CV_CH + CV_X + 512 + tid * 16) = xv[c2];
        lds_arrive(lds + L_CNTB, lane);
        if (helper) lds_wait(lds + L_CNTP, 8);
        G2W w2; g2w_init(w2, p, ldsb, cg, lane);
#pragma unroll
        for (int s = 0; s < 3; ++s) g2w_stage(w2, bw >> 1, 3 * (bw & 1) + s);
        SideIn si;
        side_early(p, lds, tb, cg, bw, lane, tag, si);
        lds_wait(lds + L_CNTB, 4);
        STAMP(3);
        ConvAcc ca;
        conv_main(lds, bw, lane, ca, w2);
        STAMP(4);
        conv_finish(p, lds, cp, bw, lane, tag, ca, cin.dch);
        STAMP(5);
        side_late(p, lds, tb, cg, bw, lane, tag, si);
        if (blockIdx.x == 0 && bw == 0) {
            for (int j = 0; j < 8; ++j) poll<2, 16>(p.ws + WS_YFLAG + j * 512, 128, tag, lane);
            if (lane == 0) st4_dev(p.ws + WS_EPOCH, tag);
        }
        STAMP(6);
        WAIT_L(0); BAR();
    } else {
        if (wid == 3) {
            touch(ldsb + L_SCR, lane * 262144, mk_rsrc(p.ws, WS_END), 0);
            const unsigned o = (lane & 7) * 262144;
            if (lane < 8) { }
            touch(ldsb + L_SCR, (lane < 8) ? o : 0x7fffffffu, mk_rsrc(p.out, T_ * D_ * 2), 0);
            touch(ldsb + L_SCR, (lane < 8) ? o : 0x7fffffffu, mk_rsrc(p.wd, F_ * D_ * 2), 0);
            touch(ldsb + L_SCR, (lane < 8) ? o : 0x7fffffffu, mk_rsrc(p.ssm_k, T_ * D_ * 2), 0);
        }
        if (!helper) {
            if (wid == 0) {
                const unsigned t = mk_tag(ld4_dev(p.ws + WS_EPOCH));
                if (lane == 0) *(LAS unsigned*)(lds + L_TAG) = t;
                WAIT_L(0);
            }
            G1Comp G;
            G.kloop(lds, wid, lane);
            STAMP(2);
            lds_arrive(lds + L_CNTA, lane);
            tag = rfl(*(LAS unsigned*)(lds + L_TAG));
            G.epilogue(p, mt, ft, wid, lane);
            STAMP(4);
            publish(p.ws + WS_HFLAG + (mt * 64 + ft * 4 + wid) * 4, tag, lane);
            STAMP(5);
        } else {
            tag = prep_phase(p, lds, xcd * 4 + (slot - 28), wid, lane STAMP_PASS);
            lds_arrive(lds + L_CNTP, lane);
            lds_arrive(lds + L_CNTA, lane);
            STAMP(2);
        }
        gemm2_main(p, lds, ldsb, tb, cg, wid, lane, tag STAMP_PASS);
        STAMP(6);
    }
    WAIT_L(0); BAR();
    final_mix(p, lds, tb, cg, wid, lane);
    STAMP(7);
    STAMP_END;
}

extern "C" void kernel_launch(void* const* d_in, const int* in_sizes, int n_in, void* d_out, int out_size, void* d_ws, size_t ws_size, hipStream_t stream) {
    Params p;
    p.q = (const char*)d_in[0]; p.k = (const char*)d_in[1]; p.v = (const char*)d_in[2]; p.shift_k = (const char*)d_in[3];
    p.ssm_k = (const char*)d_in[4]; p.ssm_D = (const char*)d_in[5];
    p.wg = (const char*)d_in[7]; p.wu = (const char*)d_in[8]; p.wd = (const char*)d_in[9]; p.gate_w = (const char*)d_in[10];
    p.out = (char*)d_out; p.ws = (char*)d_ws;
    (void)hipFuncSetAttribute((const void*)fused_kernel, hipFuncAttributeMaxDynamicSharedMemorySize, LDS_BYTES);
    fused_kernel<<<256, 512, LDS_BYTES, stream>>>(p);
}
```

```cpp
#include <hip/hip_runtime.h>
#include <hip/hip_bf16.h>
#include <stdint.h>
#include <type_traits>

#define LAS __attribute__((address_space(3)))
#define GAS __attribute__((address_space(1)))
#define DI __device__ __forceinline__
typedef __bf16 bf16x8 __attribute__((ext_vector_type(8)));
typedef __bf16 bf16x4 __attribute__((ext_vector_type(4)));
typedef __bf16 bf16x2 __attribute__((ext_vector_type(2)));
typedef float f32x4 __attribute__((ext_vector_type(4)));
typedef float f32x2 __attribute__((ext_vector_type(2)));
typedef unsigned u32x4 __attribute__((ext_vector_type(4)));
typedef unsigned u32x2 __attribute__((ext_vector_type(2)));
typedef LAS char lchar;
typedef __amdgpu_buffer_rsrc_t rsrc_t;
typedef GAS unsigned gu32;

constexpr int CODE_PF_KB = 40;
constexpr int T_ = 2048, D_ = 512, F_ = 1792;
constexpr unsigned WS_EPOCH = 0;
constexpr unsigned WS_HFLAG = 1024;
constexpr unsigned WS_KVFLAG = 8192;
constexpr unsigned WS_YFLAG = 12288;
constexpr unsigned WS_GFLAG = 20480;
constexpr unsigned WS_STAMP = 32768;
constexpr unsigned WS_GATE = 65536;
constexpr unsigned WS_KVT = 131072;
constexpr unsigned WS_Y2T = WS_KVT + D_ * T_ * 2;
constexpr unsigned WS_H = WS_Y2T + D_ * T_ * 2;
constexpr unsigned WS_END = WS_H + T_ * F_ * 2;
constexpr int G1_STAGE = 49152;
constexpr int G2_NS = 6, G2_RING = G2_NS * 4096;
constexpr int L_OST = 65536;
constexpr int CV_CP = 4160, CV_X = 4 * CV_CP, CV_CH = CV_X + 4608;
constexpr int L_CV = 98304;
constexpr int L_EX = L_CV + 2 * CV_CH;
constexpr int L_SCR = L_EX + 16384;
constexpr int L_TAG = L_SCR + 256;
constexpr int L_CNTA = L_TAG + 4, L_CNTB = L_TAG + 8, L_PAIR = L_TAG + 12, L_HRDY = L_TAG + 28, L_KVRDY = L_TAG + 32, L_YRDY = L_TAG + 36, L_RING = L_TAG + 40, L_LATE = L_TAG + 44, L_CNTP = L_TAG + 48;
constexpr int L_PSK = L_TAG + 64;
constexpr int LDS_BYTES = L_PSK + 4096;
static_assert(LDS_BYTES <= 163840, "lds");

struct Params {
    const char* q; const char* k; const char* v; const char* shift_k; const char* ssm_k; const char* ssm_D;
    const char* wg; const char* wu; const char* wd; const char* gate_w;
    char* out; char* ws;
};

#define WAIT_V(n) asm volatile("s_waitcnt vmcnt(" #n ")" ::: "memory")
#define WAIT_VN(n) asm volatile("s_waitcnt vmcnt(%0)" :: "i"(n) : "memory")
#define WAIT_L(n) asm volatile("s_waitcnt lgkmcnt(" #n ")" ::: "memory")
#define BAR() do { asm volatile("" ::: "memory"); __builtin_amdgcn_s_barrier(); asm volatile("" ::: "memory"); } while (0)
#define SCHED() __builtin_amdgcn_sched_barrier(0)
#define FENCE() asm volatile("" ::: "memory")

DI rsrc_t mk_rsrc(const void* p, unsigned bytes) { return __builtin_amdgcn_make_buffer_rsrc((void*)p, (short)0, (int)bytes, 0x00020000); }
template <int OFF> DI void dma_m0(unsigned m0, unsigned voff, rsrc_t r, unsigned soff) {
    asm volatile("s_mov_b32 m0, %0\n\ts_nop 0\n\tbuffer_load_dwordx4 %1, %2, %3 offen offset:%4 lds" :: "s"(m0), "v"(voff), "s"(r), "s"(soff), "i"(OFF) : "memory");
}
template <int OFF> DI void dma_c(unsigned voff, rsrc_t r, unsigned soff) {
    asm volatile("buffer_load_dwordx4 %0, %1, %2 offen offset:%3 lds" :: "v"(voff), "s"(r), "s"(soff), "i"(OFF) : "memory");
}
DI void touch(unsigned m0, unsigned voff, rsrc_t r, unsigned soff) {
    asm volatile("s_mov_b32 m0, %0\n\ts_nop 0\n\tbuffer_load_dword %1, %2, %3 offen lds" :: "s"(m0), "v"(voff), "s"(r), "s"(soff) : "memory");
}
DI bf16x8 lds_b128(lchar* p) { return *(LAS bf16x8*)p; }
DI bf16x4 lds_tr(lchar* p) { return __builtin_amdgcn_ds_read_tr16_b64_v4bf16((LAS bf16x4*)p); }
DI bf16x8 cat8(bf16x4 a, bf16x4 b) { return __builtin_shufflevector(a, b, 0, 1, 2, 3, 4, 5, 6, 7); }
DI f32x4 mfma(bf16x8 a, bf16x8 b, f32x4 c) { return __builtin_amdgcn_mfma_f32_16x16x32_bf16(a, b, c, 0, 0, 0); }
DI unsigned pk_bf16(float lo, float hi) { bf16x2 r; r[0] = (__bf16)lo; r[1] = (__bf16)hi; return __builtin_bit_cast(unsigned, r); }
DI float bf_lo(unsigned u) { return __builtin_bit_cast(float, u << 16); }
DI float bf_hi(unsigned u) { return __builtin_bit_cast(float, u & 0xffff0000u); }
DI u32x4 gload16(const char* p) { return *(const GAS u32x4*)p; }
DI unsigned rfl(unsigned x) { return __builtin_amdgcn_readfirstlane(x); }
DI u32x4 ld16_dev(rsrc_t r, unsigned voff, unsigned soff) { return __builtin_amdgcn_raw_buffer_load_b128(r, voff, soff, 16); }
DI void st16_dev(u32x4 v, rsrc_t r, unsigned voff, unsigned soff) { __builtin_amdgcn_raw_buffer_store_b128(v, r, voff + soff, 0, 16); }
DI unsigned ld4_dev(const char* p) { return __hip_atomic_load((gu32*)p, __ATOMIC_RELAXED, __HIP_MEMORY_SCOPE_AGENT); }
DI void st4_dev(char* p, unsigned v) { __hip_atomic_store((gu32*)p, v, __ATOMIC_RELAXED, __HIP_MEMORY_SCOPE_AGENT); }
DI void publish(char* flag, unsigned tag, int lane) { WAIT_V(0); if (lane == 0) st4_dev(flag, tag); }
template <int NV, int SLEEP = 4> DI void poll(const char* flags, int n, unsigned tag, int lane) {
    for (int it = 0; it < 200000; ++it) {
        bool ok = true;
#pragma unroll
        for (int j = 0; j < NV; ++j) { const int i = lane + 64 * j; if (i < n) ok &= (ld4_dev(flags + i * 4) == tag); }
        if (__all(ok)) break;
        __builtin_amdgcn_s_sleep(SLEEP);
    }
    FENCE();
}
DI void lds_arrive(lchar* w, int lane) { WAIT_L(0); if (lane == 0) __hip_atomic_fetch_add((LAS unsigned*)w, 1u, __ATOMIC_RELAXED, __HIP_MEMORY_SCOPE_WORKGROUP); }
template <int SLEEP = 1> DI void lds_wait(lchar* w, unsigned n) {
    for (int it = 0; it < 4000000; ++it) {
        if (rfl(__hip_atomic_load((LAS unsigned*)w, __ATOMIC_RELAXED, __HIP_MEMORY_SCOPE_WORKGROUP)) >= n) break;
        __builtin_amdgcn_s_sleep(SLEEP);
    }
    FENCE();
}
DI unsigned mk_tag(unsigned epoch) { return rfl(epoch) * 1664525u + 1013904223u; }
#define STAMP(i) do {} while (0)
#define STAMP_DECL
#define STAMP_ARG
#define STAMP_PASS
#define STAMP_END do {} while (0)
#define STAMPC(i) do {} while (0)

template <int CTRL> DI float dpp_add(float v) {
    return v + __builtin_bit_cast(float, __builtin_amdgcn_update_dpp(0, __builtin_bit_cast(int, v), CTRL, 0xf, 0xf, true));
}
DI f32x2 geglu2(f32x2 g, f32x2 u) {
    const float k0 = -2.302208198f, k1 = -0.1029432397f;
    f32x2 a = g * ((g * g) * k1 + k0);
    f32x2 e; e.x = __builtin_amdgcn_exp2f(a.x); e.y = __builtin_amdgcn_exp2f(a.y);
    e = e + 1.0f;
    f32x2 r; r.x = __builtin_amdgcn_rcpf(e.x); r.y = __builtin_amdgcn_rcpf(e.y);
    return (g * u) * r;
}
DI unsigned geglu_pk(f32x4 g, f32x4 u, int i) {
    f32x2 h = geglu2((f32x2){g[i], g[i + 1]}, (f32x2){u[i], u[i + 1]});
    return pk_bf16(h.x, h.y);
}

struct G1Load {
    unsigned vx[4], vw[4]; rsrc_t rx, rg, ru; unsigned ldsb; int lw, rot;
    DI void init(const Params& p, unsigned ldsb_, int mt, int ft, int lw_, int lane) {
        ldsb = ldsb_; lw = lw_;
        rot = ((mt + ft) & 1) * 4;
        rx = mk_rsrc(p.q + (size_t)128 * mt * 1024, 128 * 1024);
        rg = mk_rsrc(p.wg + ft * 256, D_ * F_ * 2 - ft * 256);
        ru = mk_rsrc(p.wu + ft * 256, D_ * F_ * 2 - ft * 256);
        const unsigned xoff = (lane >> 3) * 1024 + (((lane & 7) ^ (lane >> 3)) << 4) + lw * 4 * 8192;
        const unsigned woff = (lane >> 4) * (F_ * 2) + (((lane & 15) ^ (2 * (lane >> 4))) << 4) + lw * 16 * (F_ * 2);
#pragma unroll
        for (int i = 0; i < 4; ++i) { vx[i] = xoff + i * (8192 - 1024); vw[i] = (woff ^ ((i & 2) ? 128 : 0)) + i * (4 * F_ * 2 - 1024); }
    }
    DI void piece4(unsigned m0, const unsigned* v, rsrc_t r, unsigned so) {
        dma_m0<0>(m0, v[0], r, so); dma_c<1024>(v[1], r, so); dma_c<2048>(v[2], r, so); dma_c<3072>(v[3], r, so);
    }
    DI void stage(int kt0, int buf) {
        const int kt = (kt0 + rot) & 7;
        const unsigned b = ldsb + buf * G1_STAGE + lw * 4096;
        piece4(b, vx, rx, kt * 128);
        piece4(b + 16384, vw, rg, kt * 64 * F_ * 2);
        piece4(b + 32768, vw, ru, kt * 64 * F_ * 2);
    }
};

struct G1Comp {
    f32x4 ag[4][4], au[4][4];
    DI void kloop(lchar* lds, int wid, int lane) {
        const int wm = wid >> 1, wn = wid & 1;
        const int c = lane & 15, q = lane >> 4, qq = (lane & 15) >> 2, pp = lane & 3;
        const int xr0 = (64 * wm + c) * 128 + ((q ^ (c & 7)) << 4);
        const int sk = qq | ((q & 1) << 2);
        int wr[4];
#pragma unroll
        for (int n = 0; n < 4; ++n) wr[n] = 16384 + (8 * q + qq) * 256 + (((4 * wn + n) ^ sk) << 5) + ((pp >> 1) << 4) + ((pp & 1) << 3);
#pragma unroll
        for (int m = 0; m < 4; ++m)
#pragma unroll
            for (int n = 0; n < 4; ++n) { ag[m][n] = (f32x4){0, 0, 0, 0}; au[m][n] = (f32x4){0, 0, 0, 0}; }
        bf16x8 X[2][4]; bf16x4 Wh[2][8][2];
        auto rdx = [&](lchar* s, int ss, int m) { return lds_b128(s + (xr0 ^ (ss * 64)) + m * 2048); };
        auto rdw = [&](lchar* s, int ss, int j, int hh) { return lds_tr(s + wr[j & 3] + ss * 8192 + (j >> 2) * 16384 + hh * 1024); };
        auto substep = [&](auto CUR, bool rd, lchar* s, int ss) {
            constexpr int cur = CUR.value, nxt = cur ^ 1;
#pragma unroll
            for (int j = 0; j < 8; ++j) {
                const bf16x8 wf = cat8(Wh[cur][j][0], Wh[cur][j][1]);
#pragma unroll
                for (int m = 0; m < 4; ++m) {
                    if (j < 4) ag[m][j] = mfma(wf, X[cur][m], ag[m][j]);
                    else au[m][j - 4] = mfma(wf, X[cur][m], au[m][j - 4]);
                    SCHED();
                    const int g = 4 * j + m;
                    if (rd) {
                        if (g < 2) Wh[nxt][0][g] = rdw(s, ss, 0, g);
                        else if (g < 6) X[nxt][g - 2] = rdx(s, ss, g - 2);
                        else if (g < 20) Wh[nxt][1 + ((g - 6) >> 1)][(g - 6) & 1] = rdw(s, ss, 1 + ((g - 6) >> 1), (g - 6) & 1);
                    }
                    SCHED();
                }
            }
        };
        std::integral_constant<int, 0> I0; std::integral_constant<int, 1> I1;
        BAR();
#pragma unroll
        for (int m = 0; m < 4; ++m) X[0][m] = rdx(lds, 0, m);
#pragma unroll
        for (int j = 0; j < 8; ++j) { Wh[0][j][0] = rdw(lds, 0, j, 0); Wh[0][j][1] = rdw(lds, 0, j, 1); }
        SCHED();
        substep(I0, true, lds, 1);
#pragma unroll
        for (int kt = 1; kt < 8; ++kt) {
            lchar* s = lds + (kt % 3) * G1_STAGE;
            WAIT_L(0); BAR(); SCHED();
            substep(I1, true, s, 0);
            substep(I0, true, s, 1);
        }
        substep(I1, false, lds, 0);
    }
    DI void epilogue(const Params& p, int mt, int ft, int wid, int lane) {
        const int wm = wid >> 1, wn = wid & 1;
        const rsrc_t rh = mk_rsrc(p.ws + WS_H, T_ * F_ * 2);
        const unsigned hb = (unsigned)((2 * mt + wm) * 56 + 4 * ft + 2 * wn) * 4096;
#pragma unroll
        for (int j = 0; j < 2; ++j)
#pragma unroll
            for (int m = 0; m < 4; ++m) {
                u32x4 o;
                o.x = geglu_pk(ag[m][2 * j], au[m][2 * j], 0); o.y = geglu_pk(ag[m][2 * j], au[m][2 * j], 2);
                o.z = geglu_pk(ag[m][2 * j + 1], au[m][2 * j + 1], 0); o.w = geglu_pk(ag[m][2 * j + 1], au[m][2 * j + 1], 2);
                st16_dev(o, rh, lane * 16, hb + (j * 4 + m) * 1024);
            }
    }
};

DI unsigned prep_phase(const Params& p, lchar* lds, int tb, int wid, int lane STAMP_ARG) {
    const int tg = lane >> 3, cc = lane & 7;
    const int t0 = 64 * tb + 8 * tg;
    const int ch0 = 64 * wid + 8 * cc;
    u32x4 kr[11], vr[8], sr[4];
    const rsrc_t rkin = mk_rsrc(p.k, T_ * D_ * 2), rvin = mk_rsrc(p.v, T_ * D_ * 2), rs = mk_rsrc(p.shift_k + 512 * wid, 512);
    const u32x4 sk = __builtin_amdgcn_raw_buffer_load_b128(rs, (lane & 31) * 16, 0, 0);
#pragma unroll
    for (int i = 0; i < 11; ++i) kr[i] = __builtin_amdgcn_raw_buffer_load_b128(rkin, (unsigned)((t0 - 3 + i) * 1024 + ch0 * 2), 0, 0);
#pragma unroll
    for (int i = 0; i < 8; ++i) vr[i] = __builtin_amdgcn_raw_buffer_load_b128(rvin, (unsigned)((t0 + i) * 1024 + ch0 * 2), 0, 0);
    const unsigned epoch = ld4_dev(p.ws + WS_EPOCH);
    lchar* st = lds + wid * 8192;
    {
        lchar* s = lds + L_PSK + wid * 512;
        *(LAS u32x4*)(s + (lane & 31) * 16) = sk;
        WAIT_L(0);
#pragma unroll
        for (int i = 0; i < 4; ++i) sr[i] = *(LAS u32x4*)(s + cc * 64 + i * 16);
    }
#pragma unroll
    for (int j = 0; j < 4; ++j) {
        const f32x2 s0 = {bf_lo(sr[j][0]), bf_lo(sr[j][2])}, s1 = {bf_hi(sr[j][0]), bf_hi(sr[j][2])};
        const f32x2 s2 = {bf_lo(sr[j][1]), bf_lo(sr[j][3])}, s3 = {bf_hi(sr[j][1]), bf_hi(sr[j][3])};
        f32x2 kk[11], kv[8], vv[8];
#pragma unroll
        for (int r = 0; r < 11; ++r) kk[r] = (f32x2){bf_lo(kr[r][j]), bf_hi(kr[r][j])};
        SCHED();
#pragma unroll
        for (int r = 0; r < 8; ++r) kv[r] = kk[r] * s3;
        SCHED();
#pragma unroll
        for (int r = 0; r < 8; ++r) kv[r] = kk[r + 1] * s2 + kv[r];
        SCHED();
#pragma unroll
        for (int r = 0; r < 8; ++r) { kv[r] = kk[r + 2] * s1 + kv[r]; vv[r] = (f32x2){bf_lo(vr[r][j]), bf_hi(vr[r][j])}; }
        SCHED();
#pragma unroll
        for (int r = 0; r < 8; ++r) kv[r] = kk[r + 3] * s0 + kv[r];
        SCHED();
#pragma unroll
        for (int r = 0; r < 8; ++r) kv[r] = kv[r] * vv[r];
        SCHED();
        u32x4 o0, o1;
        o0.x = pk_bf16(kv[7].x, kv[6].x); o0.y = pk_bf16(kv[5].x, kv[4].x); o0.z = pk_bf16(kv[3].x, kv[2].x); o0.w = pk_bf16(kv[1].x, kv[0].x);
        o1.x = pk_bf16(kv[7].y, kv[6].y); o1.y = pk_bf16(kv[5].y, kv[4].y); o1.z = pk_bf16(kv[3].y, kv[2].y); o1.w = pk_bf16(kv[1].y, kv[0].y);
        *(LAS u32x4*)(st + (8 * cc + 2 * j) * 128 + ((tg ^ cc) << 4)) = o0;
        *(LAS u32x4*)(st + (8 * cc + 2 * j + 1) * 128 + ((tg ^ cc) << 4)) = o1;
    }
    WAIT_L(0);
    STAMP(12);
    const rsrc_t rk = mk_rsrc(p.ws + WS_KVT, D_ * T_ * 2);
#pragma unroll
    for (int i = 0; i < 8; ++i) {
        const int cl = 8 * i + (lane >> 3);
        const u32x4 o = *(LAS u32x4*)(st + cl * 128 + (((lane & 7) ^ i) << 4));
        st16_dev(o, rk, (64 * wid + cl) * 4096 + (lane & 7) * 16, tb * 128);
    }
    const unsigned tag = mk_tag(epoch);
    STAMP(13);
    publish(p.ws + WS_KVFLAG + (wid * 32 + tb) * 4, tag, lane);
    STAMP(14);
    const int i16 = lane & 15, tk = lane >> 4;
    u32x4 gw[4], xv[2][4];
    const rsrc_t rq = mk_rsrc(p.q, T_ * D_ * 2), rw = mk_rsrc(p.gate_w, D_ * 2);
#pragma unroll
    for (int j = 0; j < 4; ++j) gw[j] = __builtin_amdgcn_raw_buffer_load_b128(rw, (i16 + 16 * j) * 16, 0, 0);
#pragma unroll
    for (int g4 = 0; g4 < 2; ++g4)
#pragma unroll
        for (int j = 0; j < 4; ++j) xv[g4][j] = __builtin_amdgcn_raw_buffer_load_b128(rq, (64 * tb + 8 * wid + 4 * g4 + tk) * 1024 + (i16 + 16 * j) * 16, 0, 0);
#pragma unroll
    for (int g4 = 0; g4 < 2; ++g4) {
        f32x2 acc = {0.f, 0.f};
#pragma unroll
        for (int j = 0; j < 4; ++j)
#pragma unroll
            for (int e = 0; e < 4; ++e) acc += (f32x2){bf_lo(xv[g4][j][e]), bf_hi(xv[g4][j][e])} * (f32x2){bf_lo(gw[j][e]), bf_hi(gw[j][e])};
        float a = acc.x + acc.y;
        a = dpp_add<0x111>(a); a = dpp_add<0x112>(a); a = dpp_add<0x114>(a); a = dpp_add<0x118>(a);
        const float g = __builtin_amdgcn_rcpf(1.0f + __builtin_amdgcn_exp2f(-1.4426950408889634f * a));
        if (i16 == 15) st4_dev(p.ws + WS_GATE + (64 * tb + 8 * wid + 4 * g4 + tk) * 4, __builtin_bit_cast(unsigned, g));
    }
    publish(p.ws + WS_GFLAG + (tb * 8 + wid) * 4, tag, lane);
    return tag;
}

struct G2W { rsrc_t rw; unsigned vd[4]; unsigned ldsb; };
DI void g2w_init(G2W& w, const Params& p, unsigned ldsb, int cg, int lane) {
    w.rw = mk_rsrc(p.wd + cg * 128, F_ * D_ * 2 - cg * 128);
    w.ldsb = ldsb;
    const unsigned doff = (lane >> 3) * 1024 + (((lane & 7) ^ (2 * ((lane >> 4) & 3))) << 4);
#pragma unroll
    for (int i = 0; i < 4; ++i) w.vd[i] = doff + i * (8192 - 1024);
}
DI void g2w_stage(const G2W& w, int aw, int s) {
    const unsigned m0 = w.ldsb + aw * G2_RING + (s % G2_NS) * 4096, so = (unsigned)(14 * aw + s) * 32768;
    dma_m0<0>(m0, w.vd[0], w.rw, so); dma_c<1024>(w.vd[1], w.rw, so); dma_c<2048>(w.vd[2], w.rw, so); dma_c<3072>(w.vd[3], w.rw, so);
}

struct ConvIn { u32x4 h0[2], h1[2]; unsigned dch; };
DI void conv_load_filter(const Params& p, int cp, int tid, ConvIn& in) {
#pragma unroll
    for (int c2 = 0; c2 < 2; ++c2) {
        const rsrc_t r = mk_rsrc(p.ssm_k + (size_t)(2 * cp + c2) * 4096, 4096);
        in.h0[c2] = __builtin_amdgcn_raw_buffer_load_b128(r, tid * 16, 0, 0);
        in.h1[c2] = __builtin_amdgcn_raw_buffer_load_b128(r, tid * 16 + 16, 0, 0);
    }
    in.dch = __builtin_amdgcn_raw_buffer_load_b16(mk_rsrc(p.ssm_D, D_ * 2), (2 * cp + (tid >> 7)) * 2, 0, 0);
}
DI void conv_build_filter(lchar* lds, int tid, const ConvIn& in) {
#pragma unroll
    for (int c2 = 0; c2 < 2; ++c2) {
        lchar* base = lds + L_CV + c2 * CV_CH;
        auto emit = [&](int chunk, u32x4 a, u32x4 b) {
            u32x4 o;
            o = (u32x4){__builtin_amdgcn_alignbit(a.y, a.x, 16), __builtin_amdgcn_alignbit(a.z, a.y, 16), __builtin_amdgcn_alignbit(a.w, a.z, 16), __builtin_amdgcn_alignbit(b.x, a.w, 16)};
            *(LAS u32x4*)(base + 0 * CV_CP + chunk * 16) = o;
            o = (u32x4){a.y, a.z, a.w, b.x};
            *(LAS u32x4*)(base + 1 * CV_CP + chunk * 16) = o;
            o = (u32x4){__builtin_amdgcn_alignbit(a.z, a.y, 16), __builtin_amdgcn_alignbit(a.w, a.z, 16), __builtin_amdgcn_alignbit(b.x, a.w, 16), __builtin_amdgcn_alignbit(b.y, b.x, 16)};
            *(LAS u32x4*)(base + 2 * CV_CP + chunk * 16) = o;
            o = (u32x4){a.z, a.w, b.x, b.y};
            *(LAS u32x4*)(base + 3 * CV_CP + chunk * 16) = o;
        };
        emit(tid + 2, in.h0[c2], in.h1[c2]);
        if (tid < 2) { const u32x4 z = {0, 0, 0, 0}; emit(1 - tid, z, tid == 0 ? in.h0[c2] : z); }
        if (tid < 32) *(LAS u32x4*)(base + CV_X + tid * 16) = (u32x4){0, 0, 0, 0};
    }
}
DI void ldv_dev_asm(u32x4& d, unsigned voff, rsrc_t r, unsigned soff) {
    asm volatile("buffer_load_dwordx4 %0, %1, %2, %3 offen sc1" : "=v"(d) : "v"(voff), "s"(r), "s"(soff) : "memory");
}
struct ConvAcc { f32x4 acc[8]; };
DI void conv_main(lchar* lds, int bw, int lane, ConvAcc& A_, const G2W& w2) {
    const int c2 = bw >> 1, half = bw & 1;
    lchar* base = lds + L_CV + c2 * CV_CH;
    const int r = lane & 15, q = lane >> 4;
    lchar* ap = base + (r & 3) * CV_CP + ((r >> 2) * 4 - 8 * q + 24) * 2 + 256 * half;
    lchar* bp = base + CV_X + 512 + (16 * r - 16 + 8 * q) * 2 - 256 * half;
    f32x4* acc = A_.acc;
#pragma unroll
    for (int g = 0; g < 8; ++g) acc[g] = (f32x4){0, 0, 0, 0};
    bf16x4 Al[2][8], Ah[2][8]; bf16x8 B[2][8];
    auto rd = [&](auto S, int ww, int i) {
        constexpr int s = S.value;
        const int u = i / 3, k = i % 3;
        if (k == 0) B[s][u] = lds_b128(bp - 64 * ww + 512 * u);
        else if (k == 1) Al[s][u] = *(LAS bf16x4*)(ap + 64 * ww + 512 * u);
        else Ah[s][u] = *(LAS bf16x4*)(ap + 64 * ww + 512 * u + 8);
    };
    auto step = [&](auto CUR, int wnext) {
        constexpr int cur = CUR.value;
        std::integral_constant<int, cur ^ 1> NXT;
        int g = 0;
#pragma unroll
        for (int sum = 0; sum < 8; ++sum)
#pragma unroll
            for (int u = 0; u <= sum; ++u) {
                const int t = sum - u;
                acc[sum] = mfma(cat8(Al[cur][u], Ah[cur][u]), B[cur][t], acc[sum]);
                SCHED();
                if (wnext >= 0 && g < 24) rd(NXT, wnext, g);
                ++g;
                SCHED();
            }
    };
    std::integral_constant<int, 0> I0; std::integral_constant<int, 1> I1;
#pragma unroll
    for (int i = 0; i < 24; ++i) rd(I0, 0, i);
    SCHED();
    step(I0, 1);
    lds_wait(lds + L_CNTA, 4);
#pragma unroll
    for (int s = 0; s < 3; ++s) g2w_stage(w2, 2 + (bw >> 1), 3 * (bw & 1) + s);
    SCHED();
    step(I1, 2); step(I0, 3);
    WAIT_V(0); lds_arrive(lds + L_RING, lane);
    step(I1, -1);
#pragma unroll
    for (int j = 0; j < 4; ++j) {
        f32x4 a = acc[j], b = acc[4 + j];
        asm volatile("" : "+v"(a), "+v"(b));
        acc[j] = half ? b : a; acc[4 + j] = half ? a : b;
        *(LAS f32x4*)(lds + L_EX + bw * 4096 + j * 1024 + lane * 16) = acc[4 + j];
    }
    lds_arrive(lds + L_PAIR + bw * 4, lane);
    lds_wait(lds + L_PAIR + (bw ^ 1) * 4, 1);
}
DI void conv_finish(const Params& p, lchar* lds, int cp, int bw, int lane, unsigned tag, ConvAcc& A_, unsigned dbits) {
    const int c2 = bw >> 1, half = bw & 1;
    lchar* base = lds + L_CV + c2 * CV_CH;
    const int r = lane & 15, q = lane >> 4;
    const int ch = 2 * cp + c2;
    const float dch = bf_lo(dbits);
    lchar* stg = base + half * 2048;
#pragma unroll
    for (int j = 0; j < 4; ++j) {
        const f32x4 y = A_.acc[j] + *(LAS f32x4*)(lds + L_EX + (bw ^ 1) * 4096 + j * 1024 + lane * 16);
        const int t0 = 256 * (4 * half + j) + 16 * r + 4 * q;
        const u32x2 kv = *(LAS u32x2*)(base + CV_X + 512 + ((t0 & ~7) + 4 - (t0 & 7)) * 2);
        u32x2 o;
        o.x = pk_bf16(y[0] + bf_hi(kv.y) * dch, y[1] + bf_lo(kv.y) * dch);
        o.y = pk_bf16(y[2] + bf_hi(kv.x) * dch, y[3] + bf_lo(kv.x) * dch);
        *(LAS u32x2*)(stg + (t0 & 1023) * 2) = o;
    }
    WAIT_L(0);
    const rsrc_t ry = mk_rsrc(p.ws + WS_Y2T, D_ * T_ * 2);
#pragma unroll
    for (int i = 0; i < 2; ++i) {
        const u32x4 o = *(LAS u32x4*)(stg + i * 1024 + lane * 16);
        st16_dev(o, ry, lane * 16, ch * 4096 + half * 2048 + i * 1024);
    }
    publish(p.ws + WS_YFLAG + ((cp >> 5) * 128 + (cp & 31) * 4 + bw) * 4, tag, lane);
}

DI void ldh(bf16x8& d, unsigned voff, rsrc_t r, unsigned soff) {
    asm volatile("buffer_load_dwordx4 %0, %1, %2, %3 offen sc1" : "=v"(d) : "v"(voff), "s"(r), "s"(soff) : "memory");
}
constexpr int g2_nD(int s) { return (s >= 1 && s <= 4) ? 4 : 0; }
constexpr int g2_nH(int s) { return (s >= 0 && s <= 9) ? 4 : 0; }
template <int S> DI constexpr int g2_wait() {
    int n = 0;
    for (int s = S - 3; s <= S - 1; ++s) if (s >= 0) n += g2_nD(s) + g2_nH(s);
    if (S < 3) n += 4 * (3 - S);
    return n;
}
#define LATE_STEP 5
template <int S> DI void g2_step(lchar* lds, int lane, lchar* ring, const int (&ar)[4], const G2W& w, int aw, rsrc_t rh, unsigned hv, unsigned hs, bf16x8 (&H)[5][4], bf16x4 (&Wf)[4][4][2], f32x4 (&acc)[4][4]) {
    constexpr int cur = S & 3, hc = S % 5;
    asm volatile("s_waitcnt vmcnt(%4)" : "+v"(H[hc][0]), "+v"(H[hc][1]), "+v"(H[hc][2]), "+v"(H[hc][3]) : "i"(g2_wait<S>()) : "memory");
    SCHED();
    if (S == LATE_STEP && aw == 0 && lane == 0) *(volatile LAS unsigned*)(lds + L_LATE) = 1;
    const unsigned m0 = w.ldsb + aw * G2_RING + ((S + 9) % G2_NS) * 4096, so = (unsigned)(14 * aw + S + 9) * 32768;
#pragma unroll
    for (int n = 0; n < 4; ++n) {
        const bf16x8 wf = cat8(Wf[cur][n][0], Wf[cur][n][1]);
#pragma unroll
        for (int m = 0; m < 4; ++m) {
            acc[n][m] = mfma(wf, H[hc][m], acc[n][m]);
            SCHED();
            const int g = 4 * n + m;
            if (m == 3 && S + 4 <= 13) {
                Wf[cur][n][0] = lds_tr(ring + ((S + 4) % G2_NS) * 4096 + ar[n]);
                Wf[cur][n][1] = lds_tr(ring + ((S + 4) % G2_NS) * 4096 + ar[n] + 2048);
            }
            if (g2_nD(S)) {
                if (g == 3) { WAIT_L(2); dma_m0<0>(m0, w.vd[0], w.rw, so); }
                if (g == 5) dma_c<1024>(w.vd[1], w.rw, so);
                if (g == 7) dma_c<2048>(w.vd[2], w.rw, so);
                if (g == 9) dma_c<3072>(w.vd[3], w.rw, so);
                if (g2_nH(S) && g >= 10 && g <= 13) ldh(H[(S + 4) % 5][g - 10], hv, rh, hs + ((S + 4) * 4 + (g - 10)) * 1024);
            } else if (g2_nH(S) && (g & 3) == 1) ldh(H[(S + 4) % 5][g >> 2], hv, rh, hs + ((S + 4) * 4 + (g >> 2)) * 1024);
            SCHED();
        }
    }
}
template <int... S> DI void g2_steps(std::integer_sequence<int, S...>, lchar* lds, int lane, lchar* ring, const int (&ar)[4], const G2W& w, int aw, rsrc_t rh, unsigned hv, unsigned hs, bf16x8 (&H)[5][4], bf16x4 (&Wf)[4][4][2], f32x4 (&acc)[4][4]) {
    (g2_step<S>(lds, lane, ring, ar, w, aw, rh, hv, hs, H, Wf, acc), ...);
}
DI void gemm2_main(const Params& p, lchar* lds, unsigned ldsb, int tb, int cg, int aw, int lane, unsigned tag STAMP_ARG) {
    const int wid = aw; (void)wid;
    const int c = lane & 15, q = lane >> 4, qq = (lane & 15) >> 2, pp = lane & 3;
    G2W w; g2w_init(w, p, ldsb, cg, lane);
    lchar* ring = lds + aw * G2_RING;
    const int sw = 2 * (q & 1) + (qq >> 1);
    int ar[4];
#pragma unroll
    for (int n = 0; n < 4; ++n) ar[n] = (4 * q + qq) * 128 + ((n ^ sw) << 5) + ((pp >> 1) << 4) + ((pp & 1) << 3);
    f32x4 acc[4][4];
#pragma unroll
    for (int n = 0; n < 4; ++n)
#pragma unroll
        for (int m = 0; m < 4; ++m) acc[n][m] = (f32x4){0, 0, 0, 0};
    bf16x8 H[5][4]; bf16x4 Wf[4][4][2];
    const rsrc_t rh = mk_rsrc(p.ws + WS_H, T_ * F_ * 2);
    const unsigned hv = lane * 16, hs = (unsigned)(tb * 56 + 14 * aw) * 4096;
    lds_wait(lds + L_RING, 4);
#pragma unroll
    for (int s = 0; s < 4; ++s)
#pragma unroll
        for (int g = 0; g < 8; ++g) Wf[s][g >> 1][g & 1] = lds_tr(ring + s * 4096 + ar[g >> 1] + (g & 1) * 2048);
#pragma unroll
    for (int s = 0; s < 4; ++s)
        asm volatile("s_waitcnt lgkmcnt(0)" : "+v"(Wf[s][0][0]), "+v"(Wf[s][0][1]), "+v"(Wf[s][1][0]), "+v"(Wf[s][1][1]), "+v"(Wf[s][2][0]), "+v"(Wf[s][2][1]), "+v"(Wf[s][3][0]), "+v"(Wf[s][3][1]) :: "memory");
#pragma unroll
    for (int s = 6; s < 10; ++s) g2w_stage(w, aw, s);
    STAMP(3);
    if (aw == 0) {
        poll<1>(p.ws + WS_HFLAG + (tb >> 1) * 256, 56, tag, lane);
        lds_arrive(lds + L_HRDY, lane);
    } else lds_wait(lds + L_HRDY, 1);
    STAMP(8); STAMPC(12);
#pragma unroll
    for (int s = 0; s < 4; ++s)
#pragma unroll
        for (int m = 0; m < 4; ++m) ldh(H[s][m], hv, rh, hs + (s * 4 + m) * 1024);
    SCHED();
    g2_steps(std::make_integer_sequence<int, 14>{}, lds, lane, ring, ar, w, aw, rh, hv, hs, H, Wf, acc);
    STAMP(9); STAMPC(13);
    WAIT_L(0); BAR();
#pragma unroll
    for (int m = 0; m < 4; ++m)
#pragma unroll
        for (int n = 0; n < 4; ++n)
            *(LAS f32x4*)(lds + aw * 16384 + (16 * m + c) * 256 + (((4 * n + q) ^ c) << 4)) = acc[n][m];
}
struct SideIn { u32x4 qv[2]; };
DI void side_early(const Params& p, lchar* lds, int tb, int cg, int bw, int lane, unsigned tag, SideIn& in) {
#pragma unroll
    for (int i = 0; i < 2; ++i) in.qv[i] = gload16(p.q + (size_t)(64 * tb + 16 * bw + 8 * i + (lane >> 3)) * 1024 + cg * 128 + (lane & 7) * 16);
}
DI void side_late(const Params& p, lchar* lds, int tb, int cg, int bw, int lane, unsigned tag, const SideIn& in) {
    if (bw == 3) {
        lds_wait<8>(lds + L_LATE, 1);
        poll<1, 16>(p.ws + WS_GFLAG + tb * 32, 8, tag, lane);
        const unsigned g = ld4_dev(p.ws + WS_GATE + (64 * tb + lane) * 4);
        *(LAS unsigned*)(lds + L_SCR + lane * 4) = g;
    }
    if (bw == 0) { lds_wait<8>(lds + L_LATE, 1); poll<2, 16>(p.ws + WS_YFLAG + cg * 512, 128, tag, lane); lds_arrive(lds + L_YRDY, lane); }
    else lds_wait<8>(lds + L_YRDY, 1);
    const rsrc_t ry = mk_rsrc(p.ws + WS_Y2T, D_ * T_ * 2);
    u32x4 yv[2];
#pragma unroll
    for (int i = 0; i < 2; ++i) yv[i] = ld16_dev(ry, (64 * cg + 16 * bw + 8 * i + (lane >> 3)) * 4096 + (lane & 7) * 16, tb * 128);
    lchar* slot = lds + L_EX + (bw ^ 1) * 4096;
#pragma unroll
    for (int i = 0; i < 2; ++i) {
        const int row = 8 * i + (lane >> 3);
        *(LAS u32x4*)(slot + row * 128 + (((lane & 7) ^ (row & 7)) << 4)) = in.qv[i];
        *(LAS u32x4*)(slot + 2048 + row * 128 + (lane & 7) * 16) = yv[i];
    }
}
DI void final_mix(const Params& p, lchar* lds, int tb, int cg, int wid, int lane) {
    const int c = lane & 15, q = lane >> 4, qq = (lane & 15) >> 2, pp = lane & 3;
    const int tg = wid >> 1, ps = wid & 1;
    const int tok = 16 * tg + c, d0 = 32 * ps + 8 * q;
    const float gate = *(LAS float*)(lds + L_SCR + tok * 4);
    f32x4 b0 = {0, 0, 0, 0}, b1 = {0, 0, 0, 0};
#pragma unroll
    for (int w2 = 0; w2 < 4; ++w2) {
        b0 += *(LAS f32x4*)(lds + w2 * 16384 + tok * 256 + ((((d0 >> 2)) ^ c) << 4));
        b1 += *(LAS f32x4*)(lds + w2 * 16384 + tok * 256 + ((((d0 >> 2) + 1) ^ c) << 4));
    }
    lchar* ya = lds + L_EX + ((d0 >> 4) ^ 1) * 4096 + 2048 + ((d0 & 15) + qq) * 128 + (16 * tg + 4 * pp) * 2;
    const u32x4 yv = __builtin_bit_cast(u32x4, cat8(lds_tr(ya), lds_tr(ya + 512)));
    const u32x4 qv = *(LAS u32x4*)(lds + L_EX + (tg ^ 1) * 4096 + c * 128 + (((d0 >> 3) ^ (c & 7)) << 4));
    const float g1 = gate, g0 = 1.0f - gate;
    u32x4 o;
    o.x = pk_bf16(g1 * bf_lo(qv.x) * bf_lo(yv.x) + g0 * b0[0], g1 * bf_hi(qv.x) * bf_hi(yv.x) + g0 * b0[1]);
    o.y = pk_bf16(g1 * bf_lo(qv.y) * bf_lo(yv.y) + g0 * b0[2], g1 * bf_hi(qv.y) * bf_hi(yv.y) + g0 * b0[3]);
    o.z = pk_bf16(g1 * bf_lo(qv.z) * bf_lo(yv.z) + g0 * b1[0], g1 * bf_hi(qv.z) * bf_hi(yv.z) + g0 * b1[1]);
    o.w = pk_bf16(g1 * bf_lo(qv.w) * bf_lo(yv.w) + g0 * b1[2], g1 * bf_hi(qv.w) * bf_hi(yv.w) + g0 * b1[3]);
    lchar* stg = lds + L_OST + wid * 1024;
    *(LAS u32x4*)(stg + c * 64 + ((q ^ ((c >> 1) & 3)) << 4)) = o;
    WAIT_L(0);
    const int r2 = lane >> 2, k2 = lane & 3;
    const u32x4 o2 = *(LAS u32x4*)(stg + r2 * 64 + ((k2 ^ ((r2 >> 1) & 3)) << 4));
    *(GAS u32x4*)(p.out + (size_t)(64 * tb + 16 * tg + r2) * 1024 + cg * 128 + ps * 64 + k2 * 16) = o2;
}

__global__ void __launch_bounds__(512, 2) fused_kernel(Params p) {
    extern __shared__ __attribute__((aligned(16))) char smem[];
    lchar* lds = (lchar*)smem;
    const unsigned ldsb = (unsigned)(size_t)lds;
    const int wid = rfl(threadIdx.x >> 6), lane = threadIdx.x & 63;
    const int xcd = blockIdx.x & 7, slot = blockIdx.x >> 3;
    const bool helper = slot >= 28;
    const int fq = xcd >> 1, mh = xcd & 1;
    const int fb = (fq >> 1) * 7 + (fq & 1) * 4;
    const int ft = slot < 24 ? fb + (slot >> 3) : (fq >> 1) * 7 + 3;
    const int mt = slot < 24 ? 8 * mh + (slot & 7) : 8 * mh + 4 * (fq & 1) + (slot - 24);
    const int tb = 16 * mh + 4 * fq + (slot >> 3), cg = slot & 7;
    const int cp = blockIdx.x;
    unsigned tag;
    STAMP_DECL
    STAMP(0);
    if (wid == 0 && lane > 0 && lane < 16) *(LAS unsigned*)(lds + L_TAG + lane * 4) = 0;
    if (slot == 28 && wid < CODE_PF_KB / 8) {
        const char* pc = (const char*)(__builtin_amdgcn_s_getpc() & ~(unsigned long long)255);
        touch(ldsb + L_SCR, lane * 128, mk_rsrc(pc, CODE_PF_KB * 1024), wid * 8192);
    }
    if (slot == 29 && wid < 4) {
        const unsigned o = (lane < 8) ? lane * 262144u : 0x7fffffffu;
        if (wid == 0) touch(ldsb + L_SCR, lane * 262144, mk_rsrc(p.ws, WS_END), 0);
        if (wid == 1) touch(ldsb + L_SCR, o, mk_rsrc(p.out, T_ * D_ * 2), 0);
        if (wid == 2) touch(ldsb + L_SCR, o, mk_rsrc(p.wd, F_ * D_ * 2), 0);
        if (wid == 3) touch(ldsb + L_SCR, o, mk_rsrc(p.ssm_k, T_ * D_ * 2), 0);
    }
    if (__builtin_expect(helper, 0)) { WAIT_L(0); BAR(); }
    if (__builtin_expect(wid >= 4, 1)) {
        const int bw = wid - 4, tid = bw * 64 + lane;
        ConvIn cin;
        u32x4 xv[2];
        const rsrc_t rkv = mk_rsrc(p.ws + WS_KVT, D_ * T_ * 2);
        bool kv_ok;
        if (__builtin_expect(!helper, 1)) {
            G1Load L; L.init(p, ldsb, mt, ft, bw, lane);
            asm volatile("s_nop 4");
            L.stage(0, 0); L.stage(1, 1);
#pragma unroll
            for (int kt = 0; kt < 8; ++kt) {
                if (kt <= 5) WAIT_V(12); else if (kt == 6) WAIT_V(17); else WAIT_V(2);
                BAR();
                if (kt == 0) STAMP(1);
                if (kt + 2 < 8) L.stage(kt + 2, (kt + 2) % 3);
                if (kt == 5) { conv_load_filter(p, cp, tid, cin); FENCE(); }
                if (kt == 6) {
                    tag = rfl(*(LAS unsigned*)(lds + L_TAG));
                    const unsigned f = ld4_dev(p.ws + WS_KVFLAG + (cp >> 5) * 128 + (lane & 31) * 4);
                    conv_build_filter(lds, tid, cin);
                    FENCE();
                    kv_ok = __all(f == tag);
                    if (kv_ok) { xv[0] = ld16_dev(rkv, tid * 16, (2 * cp) * 4096); xv[1] = ld16_dev(rkv, tid * 16, (2 * cp + 1) * 4096); }
                    FENCE();
                }
            }
        } else {
            tag = prep_phase(p, lds, xcd * 4 + (slot - 28), wid, lane STAMP_PASS);
            lds_arrive(lds + L_CNTP, lane);
            conv_load_filter(p, cp, tid, cin);
            conv_build_filter(lds, tid, cin);
            kv_ok = false;
        }
        STAMP(2);
        if (!kv_ok) {
            poll<1, 2>(p.ws + WS_KVFLAG + (cp >> 5) * 128, 32, tag, lane);
            xv[0] = ld16_dev(rkv, tid * 16, (2 * cp) * 4096); xv[1] = ld16_dev(rkv, tid * 16, (2 * cp + 1) * 4096);
        }
#pragma unroll
        for (int c2 = 0; c2 < 2; ++c2) *(LAS u32x4*)(lds + L_CV + c2 * CV_CH + CV_X + 512 + tid * 16) = xv[c2];
        lds_arrive(lds + L_CNTB, lane);
        if (helper) lds_wait(lds + L_CNTP, 8);
        G2W w2; g2w_init(w2, p, ldsb, cg, lane);
#pragma unroll
        for (int s = 0; s < 3; ++s) g2w_stage(w2, bw >> 1, 3 * (bw & 1) + s);
        SideIn si;
        side_early(p, lds, tb, cg, bw, lane, tag, si);
        lds_wait(lds + L_CNTB, 4);
        STAMP(3);
        ConvAcc ca;
        conv_main(lds, bw, lane, ca, w2);
        STAMP(4);
        conv_finish(p, lds, cp, bw, lane, tag, ca, cin.dch);
        STAMP(5);
        side_late(p, lds, tb, cg, bw, lane, tag, si);
        if (blockIdx.x == 0 && bw == 0) {
            for (int j = 0; j < 8; ++j) poll<2, 16>(p.ws + WS_YFLAG + j * 512, 128, tag, lane);
            if (lane == 0) st4_dev(p.ws + WS_EPOCH, tag);
        }
        STAMP(6);
        WAIT_L(0); BAR();
    } else {
        if (!helper) {
            if (wid == 0) {
                const unsigned t = mk_tag(ld4_dev(p.ws + WS_EPOCH));
                if (lane == 0) *(LAS unsigned*)(lds + L_TAG) = t;
                WAIT_L(0);
            }
            G1Comp G;
            G.kloop(lds, wid, lane);
            STAMP(2);
            lds_arrive(lds + L_CNTA, lane);
            tag = rfl(*(LAS unsigned*)(lds + L_TAG));
            G.epilogue(p, mt, ft, wid, lane);
            STAMP(4);
            publish(p.ws + WS_HFLAG + (mt * 64 + ft * 4 + wid) * 4, tag, lane);
            STAMP(5);
        } else {
            tag = prep_phase(p, lds, xcd * 4 + (slot - 28), wid, lane STAMP_PASS);
            lds_arrive(lds + L_CNTP, lane);
            lds_arrive(lds + L_CNTA, lane);
            STAMP(2);
        }
        gemm2_main(p, lds, ldsb, tb, cg, wid, lane, tag STAMP_PASS);
        STAMP(6);
    }
    WAIT_L(0); BAR();
    final_mix(p, lds, tb, cg, wid, lane);
    STAMP(7);
    STAMP_END;
}

extern "C" void kernel_launch(void* const* d_in, const int* in_sizes, int n_in, void* d_out, int out_size, void* d_ws, size_t ws_size, hipStream_t stream) {
    Params p;
    p.q = (const char*)d_in[0]; p.k = (const char*)d_in[1]; p.v = (const char*)d_in[2]; p.shift_k = (const char*)d_in[3];
    p.ssm_k = (const char*)d_in[4]; p.ssm_D = (const char*)d_in[5];
    p.wg = (const char*)d_in[7]; p.wu = (const char*)d_in[8]; p.wd = (const char*)d_in[9]; p.gate_w = (const char*)d_in[10];
    p.out = (char*)d_out; p.ws = (char*)d_ws;
    (void)hipFuncSetAttribute((const void*)fused_kernel, hipFuncAttributeMaxDynamicSharedMemorySize, LDS_BYTES);
    fused_kernel<<<256, 512, LDS_BYTES, stream>>>(p);
}
```

```cpp
#include <hip/hip_runtime.h>
#include <hip/hip_bf16.h>
#include <stdint.h>
#include <type_traits>
#include <utility>

#define LAS __attribute__((address_space(3)))
#define GAS __attribute__((address_space(1)))
#define DI __device__ __forceinline__
typedef __bf16 bf16x8 __attribute__((ext_vector_type(8)));
typedef __bf16 bf16x4 __attribute__((ext_vector_type(4)));
typedef __bf16 bf16x2 __attribute__((ext_vector_type(2)));
typedef float f32x4 __attribute__((ext_vector_type(4)));
typedef float f32x2 __attribute__((ext_vector_type(2)));
typedef unsigned u32x4 __attribute__((ext_vector_type(4)));
typedef unsigned u32x2 __attribute__((ext_vector_type(2)));
typedef LAS char lchar;
typedef __amdgpu_buffer_rsrc_t rsrc_t;
typedef GAS unsigned gu32;

constexpr int GELU_SIG = 4;
constexpr int LATE_STEP = 5;
constexpr int PF_K0 = 2;
constexpr int SSM_K = 1;
constexpr int CODE_PF_KB = 30;

constexpr int T_ = 2048, D_ = 512, F_ = 1792;
constexpr unsigned WS_EPOCH = 0;
constexpr unsigned WS_HFLAG = 1024;
constexpr unsigned WS_KVFLAG = 8192;
constexpr unsigned WS_YFLAG = 12288;
constexpr unsigned WS_GFLAG = 20480;
constexpr unsigned WS_GATE = 65536;
constexpr unsigned WS_KVT = 131072;
constexpr unsigned WS_Y2T = WS_KVT + D_ * T_ * 2;
constexpr unsigned WS_H = WS_Y2T + D_ * T_ * 2;
constexpr unsigned WS_END = WS_H + T_ * F_ * 2;
constexpr int G1_STAGE = 49152;
constexpr int G2_NS = 6;
constexpr int CV_CP = 4160, CV_X = 4 * CV_CP, CV_CH = CV_X + 4608;
constexpr int L_CV = 98304;
constexpr int L_OST = L_CV;
constexpr int L_QY = L_CV + 2 * CV_CH;
constexpr int L_GATE = L_QY + 16384;
constexpr int L_SINK = L_GATE + 256;
constexpr int L_CTL = L_SINK + 256;
constexpr int L_TAG = L_CTL, L_CNTA = L_CTL + 4, L_CVRDY = L_CTL + 8, L_HRDY = L_CTL + 12, L_YRDY = L_CTL + 16, L_RING = L_CTL + 20, L_LATE = L_CTL + 24,
              L_CNTP = L_CTL + 28, L_GELU = L_CTL + 32, L_YPUB = L_CTL + 36;
constexpr int L_PSK = L_CTL + 64;
constexpr int LDS_BYTES = L_PSK + 4096;
static_assert(LDS_BYTES <= 163840, "lds");
constexpr int ring_off(int slot) { return slot < 3 ? slot * 4096 : 49152 + (slot - 3) * 4096; }

struct Params {
    const char* q; const char* k; const char* v; const char* shift_k; const char* ssm_k; const char* ssm_D;
    const char* wg; const char* wu; const char* wd; const char* gate_w;
    char* out; char* ws;
};

#define WAIT_V(n) asm volatile("s_waitcnt vmcnt(" #n ")" ::: "memory")
#define WAIT_L(n) asm volatile("s_waitcnt lgkmcnt(" #n ")" ::: "memory")
#define BAR() do { asm volatile("" ::: "memory"); __builtin_amdgcn_s_barrier(); asm volatile("" ::: "memory"); } while (0)
#define SCHED() __builtin_amdgcn_sched_barrier(0)
#define FENCE() asm volatile("" ::: "memory")

DI rsrc_t mk_rsrc(const void* p, unsigned bytes) { return __builtin_amdgcn_make_buffer_rsrc((void*)p, (short)0, (int)bytes, 0x00020000); }
template <int OFF> DI void dma_m0(unsigned m0, unsigned voff, rsrc_t r, unsigned soff) {
    asm volatile("s_mov_b32 m0, %0\n\ts_nop 0\n\tbuffer_load_dwordx4 %1, %2, %3 offen offset:%4 lds" :: "s"(m0), "v"(voff), "s"(r), "s"(soff), "i"(OFF) : "memory");
}
template <int OFF> DI void dma_c(unsigned voff, rsrc_t r, unsigned soff) {
    asm volatile("buffer_load_dwordx4 %0, %1, %2 offen offset:%3 lds" :: "v"(voff), "s"(r), "s"(soff), "i"(OFF) : "memory");
}
DI void dma4(unsigned m0, const unsigned (&v)[4], rsrc_t r, unsigned so) {
    dma_m0<0>(m0, v[0], r, so); dma_c<1024>(v[1], r, so); dma_c<2048>(v[2], r, so); dma_c<3072>(v[3], r, so);
}
DI void touch(unsigned m0, unsigned voff, rsrc_t r) {
    asm volatile("s_mov_b32 m0, %0\n\ts_nop 0\n\tbuffer_load_dword %1, %2, 0 offen lds" :: "s"(m0), "v"(voff), "s"(r) : "memory");
}
DI bf16x8 lds_b128(lchar* p) { return *(LAS bf16x8*)p; }
DI bf16x4 lds_tr(lchar* p) { return __builtin_amdgcn_ds_read_tr16_b64_v4bf16((LAS bf16x4*)p); }
DI bf16x8 cat8(bf16x4 a, bf16x4 b) { return __builtin_shufflevector(a, b, 0, 1, 2, 3, 4, 5, 6, 7); }
DI f32x4 mfma(bf16x8 a, bf16x8 b, f32x4 c) { return __builtin_amdgcn_mfma_f32_16x16x32_bf16(a, b, c, 0, 0, 0); }
DI unsigned pk_bf16(float lo, float hi) { bf16x2 r; r[0] = (__bf16)lo; r[1] = (__bf16)hi; return __builtin_bit_cast(unsigned, r); }
DI float bf_lo(unsigned u) { return __builtin_bit_cast(float, u << 16); }
DI float bf_hi(unsigned u) { return __builtin_bit_cast(float, u & 0xffff0000u); }
DI unsigned rfl(unsigned x) { return __builtin_amdgcn_readfirstlane(x); }
DI u32x4 ld16(rsrc_t r, unsigned voff, unsigned soff = 0) { return __builtin_amdgcn_raw_buffer_load_b128(r, voff, soff, 0); }
DI u32x4 ld16_dev(rsrc_t r, unsigned voff, unsigned soff) { return __builtin_amdgcn_raw_buffer_load_b128(r, voff, soff, 16); }
DI void st16_dev(u32x4 v, rsrc_t r, unsigned off) { __builtin_amdgcn_raw_buffer_store_b128(v, r, off, 0, 16); }
DI unsigned ld4_dev(const char* p) { return __hip_atomic_load((gu32*)p, __ATOMIC_RELAXED, __HIP_MEMORY_SCOPE_AGENT); }
DI void st4_dev(char* p, unsigned v) { __hip_atomic_store((gu32*)p, v, __ATOMIC_RELAXED, __HIP_MEMORY_SCOPE_AGENT); }
DI void publish(char* flag, unsigned tag, int lane) { WAIT_V(0); if (lane == 0) st4_dev(flag, tag); }
template <int NV, int SLEEP> DI void poll(const char* flags, int n, unsigned tag, int lane) {
#pragma unroll 1
    for (int it = 0; it < 100000; ++it) {
        bool ok = true;
#pragma unroll
        for (int j = 0; j < NV; ++j) { const int i = lane + 64 * j; if (i < n) ok &= (ld4_dev(flags + i * 4) == tag); }
        if (__all(ok)) break;
        __builtin_amdgcn_s_sleep(SLEEP);
    }
    FENCE();
}
DI void lds_arrive(lchar* w, int lane) { WAIT_L(0); if (lane == 0) __hip_atomic_fetch_add((LAS unsigned*)w, 1u, __ATOMIC_RELAXED, __HIP_MEMORY_SCOPE_WORKGROUP); }
template <int SLEEP = 1> DI void lds_wait(lchar* w, unsigned n) {
#pragma unroll 1
    for (int it = 0; it < 4000000; ++it) {
        if (rfl(__hip_atomic_load((LAS unsigned*)w, __ATOMIC_RELAXED, __HIP_MEMORY_SCOPE_WORKGROUP)) >= n) break;
        __builtin_amdgcn_s_sleep(SLEEP);
    }
    FENCE();
}
DI unsigned mk_tag(unsigned epoch) { return rfl(epoch) * 1664525u + 1013904223u; }
template <class F, int... I> DI void for_seq(F f, std::integer_sequence<int, I...>) { (f(std::integral_constant<int, I>{}), ...); }
template <int CTRL> DI float dpp_add(float v) {
    return v + __builtin_bit_cast(float, __builtin_amdgcn_update_dpp(0, __builtin_bit_cast(int, v), CTRL, 0xf, 0xf, true));
}

struct G2W { rsrc_t rw; unsigned vd[4]; unsigned ldsb; int rot; };
DI int g2_kb(int rot, int aw, int s) { const int t = s + rot; return 14 * aw + (t >= 14 ? t - 14 : t); }
DI void g2w_init(G2W& w, const Params& p, unsigned ldsb, int tb, int cg, int lane) {
    { const int r = 2 * cg + 4 * (tb & 3); w.rot = r >= 14 ? (r >= 28 ? 0 : r - 14) : r; }
    w.rw = mk_rsrc(p.wd + cg * 128, F_ * D_ * 2 - cg * 128);
    w.ldsb = ldsb;
    const unsigned doff = (lane >> 3) * 1024 + (((lane & 7) ^ (2 * ((lane >> 4) & 3))) << 4);
#pragma unroll
    for (int i = 0; i < 4; ++i) w.vd[i] = doff + i * (8192 - 1024);
}
DI void g2w_stage(const G2W& w, int aw, int slot_off, int s) { dma4(w.ldsb + aw * 12288 + slot_off, w.vd, w.rw, (unsigned)g2_kb(w.rot, aw, s) * 32768); }

struct G1Load {
    unsigned v[4]; rsrc_t r; unsigned ldsb, kstep, gstep; int rot;
    DI void init(const Params& p, unsigned ldsb_, int mt, int ft, int lw, int lane) {
        ldsb = ldsb_ + lw * 16384;
        rot = ((mt + ft) & 1) * 4;
        if (lw == 0) {
            r = mk_rsrc(p.q + (size_t)128 * mt * 1024, 128 * 1024);
            const unsigned xoff = (lane >> 3) * 1024 + (((lane & 7) ^ (lane >> 3)) << 4);
#pragma unroll
            for (int i = 0; i < 4; ++i) v[i] = xoff + i * (8192 - 1024);
            kstep = 128; gstep = 4 * 8192;
        } else {
            r = mk_rsrc((lw == 1 ? p.wg : p.wu) + ft * 256, D_ * F_ * 2 - ft * 256);
            const unsigned woff = (lane >> 4) * (F_ * 2) + (((lane & 15) ^ (2 * (lane >> 4))) << 4);
#pragma unroll
            for (int i = 0; i < 4; ++i) v[i] = (woff ^ ((i & 2) ? 128 : 0)) + i * (4 * F_ * 2 - 1024);
            kstep = 64 * F_ * 2; gstep = 16 * F_ * 2;
        }
    }
    DI void stage(int kt0, int buf) {
        const unsigned b = ldsb + buf * G1_STAGE, so = ((kt0 + rot) & 7) * kstep;
#pragma unroll
        for (int g = 0; g < 4; ++g) dma4(b + g * 4096, v, r, so + g * gstep);
    }
};
DI f32x2 geglu2(f32x2 g, f32x2 u) {
    const float k0 = -2.302208198f, k1 = -0.1029432397f;
    f32x2 a = g * ((g * g) * k1 + k0);
    f32x2 e; e.x = __builtin_amdgcn_exp2f(a.x); e.y = __builtin_amdgcn_exp2f(a.y);
    e = e + 1.0f;
    f32x2 r; r.x = __builtin_amdgcn_rcpf(e.x); r.y = __builtin_amdgcn_rcpf(e.y);
    return (g * u) * r;
}
DI unsigned geglu_pk(f32x4 g, f32x4 u, int i) {
    f32x2 h = geglu2((f32x2){g[i], g[i + 1]}, (f32x2){u[i], u[i + 1]});
    return pk_bf16(h.x, h.y);
}
struct G1Comp {
    f32x4 ag[4][4], au[4][4];
    DI void kloop(lchar* lds, int wid, int lane) {
        const int wm = wid >> 1, wn = wid & 1;
        const int c = lane & 15, q = lane >> 4, qq = (lane & 15) >> 2, pp = lane & 3;
        const int xr0 = (64 * wm + c) * 128 + ((q ^ (c & 7)) << 4);
        const int sk = qq | ((q & 1) << 2);
        lchar* xa[2]; lchar* wa[4];
        xa[0] = lds + xr0; xa[1] = lds + (xr0 ^ 64);
#pragma unroll
        for (int n = 0; n < 4; ++n) wa[n] = lds + 16384 + (8 * q + qq) * 256 + (((4 * wn + n) ^ sk) << 5) + ((pp >> 1) << 4) + ((pp & 1) << 3);
#pragma unroll
        for (int m = 0; m < 4; ++m)
#pragma unroll
            for (int n = 0; n < 4; ++n) { ag[m][n] = (f32x4){0, 0, 0, 0}; au[m][n] = (f32x4){0, 0, 0, 0}; }
        bf16x8 X[2][4]; bf16x4 Wh[2][8][2];
        auto rdx = [&](int ss, int m) { return lds_b128(xa[ss] + m * 2048); };
        auto rdw = [&](int ss, int j, int hh) { return lds_tr(wa[j & 3] + ss * 8192 + (j >> 2) * 16384 + hh * 1024); };
        auto substep = [&](auto CUR, bool rd, int ss) {
            constexpr int cur = CUR.value, nxt = cur ^ 1;
#pragma unroll
            for (int j = 0; j < 8; ++j) {
                const bf16x8 wf = cat8(Wh[cur][j][0], Wh[cur][j][1]);
#pragma unroll
                for (int m = 0; m < 4; ++m) {
                    if (j < 4) ag[m][j] = mfma(wf, X[cur][m], ag[m][j]);
                    else au[m][j - 4] = mfma(wf, X[cur][m], au[m][j - 4]);
                    SCHED();
                    const int g = 4 * j + m;
                    if (rd) {
                        if (g < 2) Wh[nxt][0][g] = rdw(ss, 0, g);
                        else if (g < 6) X[nxt][g - 2] = rdx(ss, g - 2);
                        else if (g < 20) Wh[nxt][1 + ((g - 6) >> 1)][(g - 6) & 1] = rdw(ss, 1 + ((g - 6) >> 1), (g - 6) & 1);
                    }
                    SCHED();
                }
            }
        };
        std::integral_constant<int, 0> I0; std::integral_constant<int, 1> I1;
        BAR();
#pragma unroll
        for (int m = 0; m < 4; ++m) X[0][m] = rdx(0, m);
#pragma unroll
        for (int j = 0; j < 8; ++j) { Wh[0][j][0] = rdw(0, j, 0); Wh[0][j][1] = rdw(0, j, 1); }
        SCHED();
        substep(I0, true, 1);
        int buf = 0;
#pragma unroll 1
        for (int kt = 1; kt < 8; ++kt) {
            const int step = buf == 2 ? -2 * G1_STAGE : G1_STAGE;
            buf = buf == 2 ? 0 : buf + 1;
            xa[0] += step; xa[1] += step;
#pragma unroll
            for (int n = 0; n < 4; ++n) wa[n] += step;
            WAIT_L(0); BAR(); SCHED();
            substep(I1, true, 0);
            substep(I0, true, 1);
        }
        substep(I1, false, 0);
    }
    DI void epilogue(const Params& p, lchar* lds, int mt, int ft, int wid, int lane) {
        const int wm = wid >> 1, wn = wid & 1;
        const rsrc_t rh = mk_rsrc(p.ws + WS_H, T_ * F_ * 2);
        const unsigned hb = (unsigned)((2 * mt + wm) * 56 + 4 * ft + 2 * wn) * 4096 + lane * 16;
#pragma unroll
        for (int blk = 0; blk < 8; ++blk) {
            const int j = blk >> 2, m = blk & 3;
            u32x4 o;
            o.x = geglu_pk(ag[m][2 * j], au[m][2 * j], 0); o.y = geglu_pk(ag[m][2 * j], au[m][2 * j], 2);
            o.z = geglu_pk(ag[m][2 * j + 1], au[m][2 * j + 1], 0); o.w = geglu_pk(ag[m][2 * j + 1], au[m][2 * j + 1], 2);
            st16_dev(o, rh, hb + blk * 1024);
            if (blk == GELU_SIG && lane == 0) __hip_atomic_fetch_add((LAS unsigned*)(lds + L_GELU), 1u, __ATOMIC_RELAXED, __HIP_MEMORY_SCOPE_WORKGROUP);
        }
    }
};

template <class HOOK> DI unsigned prep_kv(const Params& p, lchar* lds, int tb, int wid, int lane, HOOK hook) {
    const int tg = lane >> 3, cc = lane & 7;
    const int t0 = 64 * tb + 8 * tg;
    const int ch0 = 64 * wid + 8 * cc;
    u32x4 kr[11], vr[8], sr[4];
    const rsrc_t rkin = mk_rsrc(p.k, T_ * D_ * 2), rvin = mk_rsrc(p.v, T_ * D_ * 2), rs = mk_rsrc(p.shift_k + 512 * wid, 512);
    const u32x4 sk = ld16(rs, (lane & 31) * 16);
#pragma unroll
    for (int i = 0; i < 11; ++i) kr[i] = ld16(rkin, (unsigned)((t0 - 3 + i) * 1024 + ch0 * 2));
#pragma unroll
    for (int i = 0; i < 8; ++i) vr[i] = ld16(rvin, (unsigned)((t0 + i) * 1024 + ch0 * 2));
    const unsigned epoch = ld4_dev(p.ws + WS_EPOCH);
    FENCE(); hook(); FENCE();
    lchar* st = lds + wid * 8192;
    {
        lchar* s = lds + L_PSK + wid * 512;
        *(LAS u32x4*)(s + (lane & 31) * 16) = sk;
        WAIT_L(0);
#pragma unroll
        for (int i = 0; i < 4; ++i) sr[i] = *(LAS u32x4*)(s + cc * 64 + i * 16);
    }
#pragma unroll
    for (int j = 0; j < 4; ++j) {
        const f32x2 s0 = {bf_lo(sr[j][0]), bf_lo(sr[j][2])}, s1 = {bf_hi(sr[j][0]), bf_hi(sr[j][2])};
        const f32x2 s2 = {bf_lo(sr[j][1]), bf_lo(sr[j][3])}, s3 = {bf_hi(sr[j][1]), bf_hi(sr[j][3])};
        f32x2 kk[11], kv[8], vv[8];
#pragma unroll
        for (int r = 0; r < 11; ++r) kk[r] = (f32x2){bf_lo(kr[r][j]), bf_hi(kr[r][j])};
        SCHED();
#pragma unroll
        for (int r = 0; r < 8; ++r) kv[r] = kk[r] * s3;
        SCHED();
#pragma unroll
        for (int r = 0; r < 8; ++r) kv[r] = kk[r + 1] * s2 + kv[r];
        SCHED();
#pragma unroll
        for (int r = 0; r < 8; ++r) { kv[r] = kk[r + 2] * s1 + kv[r]; vv[r] = (f32x2){bf_lo(vr[r][j]), bf_hi(vr[r][j])}; }
        SCHED();
#pragma unroll
        for (int r = 0; r < 8; ++r) kv[r] = kk[r + 3] * s0 + kv[r];
        SCHED();
#pragma unroll
        for (int r = 0; r < 8; ++r) kv[r] = kv[r] * vv[r];
        SCHED();
        u32x4 o0, o1;
        o0.x = pk_bf16(kv[7].x, kv[6].x); o0.y = pk_bf16(kv[5].x, kv[4].x); o0.z = pk_bf16(kv[3].x, kv[2].x); o0.w = pk_bf16(kv[1].x, kv[0].x);
        o1.x = pk_bf16(kv[7].y, kv[6].y); o1.y = pk_bf16(kv[5].y, kv[4].y); o1.z = pk_bf16(kv[3].y, kv[2].y); o1.w = pk_bf16(kv[1].y, kv[0].y);
        *(LAS u32x4*)(st + (8 * cc + 2 * j) * 128 + ((tg ^ cc) << 4)) = o0;
        *(LAS u32x4*)(st + (8 * cc + 2 * j + 1) * 128 + ((tg ^ cc) << 4)) = o1;
    }
    WAIT_L(0);
    const rsrc_t rk = mk_rsrc(p.ws + WS_KVT, D_ * T_ * 2);
#pragma unroll
    for (int i = 0; i < 8; ++i) {
        const int cl = 8 * i + (lane >> 3);
        const u32x4 o = *(LAS u32x4*)(st + cl * 128 + (((lane & 7) ^ i) << 4));
        st16_dev(o, rk, (64 * wid + cl) * 4096 + (lane & 7) * 16 + tb * 128);
    }
    const unsigned tag = mk_tag(epoch);
    publish(p.ws + WS_KVFLAG + (wid * 32 + tb) * 4, tag, lane);
    return tag;
}
DI void gate_phase(const Params& p, int tb, int wid, int lane, unsigned tag) {
    const int i16 = lane & 15, tk = lane >> 4;
    u32x4 gw[4], xv[4][4];
    const rsrc_t rq = mk_rsrc(p.q, T_ * D_ * 2), rw = mk_rsrc(p.gate_w, D_ * 2);
#pragma unroll
    for (int j = 0; j < 4; ++j) gw[j] = ld16(rw, (i16 + 16 * j) * 16);
#pragma unroll
    for (int g4 = 0; g4 < 4; ++g4)
#pragma unroll
        for (int j = 0; j < 4; ++j) xv[g4][j] = ld16(rq, (64 * tb + 16 * wid + 4 * g4 + tk) * 1024 + (i16 + 16 * j) * 16);
#pragma unroll
    for (int g4 = 0; g4 < 4; ++g4) {
        f32x2 acc = {0.f, 0.f};
#pragma unroll
        for (int j = 0; j < 4; ++j)
#pragma unroll
            for (int e = 0; e < 4; ++e) acc += (f32x2){bf_lo(xv[g4][j][e]), bf_hi(xv[g4][j][e])} * (f32x2){bf_lo(gw[j][e]), bf_hi(gw[j][e])};
        float a = acc.x + acc.y;
        a = dpp_add<0x111>(a); a = dpp_add<0x112>(a); a = dpp_add<0x114>(a); a = dpp_add<0x118>(a);
        const float g = __builtin_amdgcn_rcpf(1.0f + __builtin_amdgcn_exp2f(-1.4426950408889634f * a));
        if (i16 == 15) st4_dev(p.ws + WS_GATE + (64 * tb + 16 * wid + 4 * g4 + tk) * 4, __builtin_bit_cast(unsigned, g));
    }
    publish(p.ws + WS_GFLAG + (tb * 8 + wid) * 4, tag, lane);
}

DI void conv_filter_dma(const Params& p, unsigned ldsb, int cp, int lane) {
#pragma unroll
    for (int c2 = 0; c2 < 2; ++c2) {
        const rsrc_t r = mk_rsrc(p.ssm_k + (size_t)(2 * cp + c2) * 4096, 4096);
#pragma unroll
        for (int b = 0; b < 4; ++b) {
            const unsigned v0 = lane * 16 + 2 * (b + 1);
            const unsigned m0 = ldsb + L_CV + c2 * CV_CH + b * CV_CP + 32;
            dma_m0<0>(m0, v0, r, 0); dma_c<1024>(v0, r, 0); dma_c<2048>(v0, r, 0); dma_c<3072>(v0, r, 0);
        }
    }
}
struct ConvFix { u32x2 head; unsigned tail; };
DI void conv_fix_load(const Params& p, int cp, int lane, ConvFix& f) {
    const rsrc_t r = mk_rsrc(p.ssm_k + (size_t)(2 * cp) * 4096, 8192);
    const unsigned ro = ((lane >> 2) & 1) * 4096;
    f.head = __builtin_amdgcn_raw_buffer_load_b64(r, ro, 0, 0);
    f.tail = __builtin_amdgcn_raw_buffer_load_b32(r, ro + 4092, 0, 0);
}
DI void conv_fix_write(lchar* lds, int lane, const ConvFix& f) {
    if (lane < 8) {
        const int b = lane & 3;
        lchar* base = lds + L_CV + (lane >> 2) * CV_CH;
        lchar* cpb = base + b * CV_CP;
        const unsigned long long h = ((unsigned long long)f.head.y << 32) | f.head.x;
        const unsigned long long top = h << (16 * (3 - b));
        *(LAS u32x4*)cpb = (u32x4){0, 0, 0, 0};
        *(LAS u32x4*)(cpb + 16) = (u32x4){0, 0, (unsigned)top, (unsigned)(top >> 32)};
        if (!(b & 1)) *(LAS unsigned short*)(cpb + (2062 - b) * 2) = (unsigned short)(f.tail >> 16);
    }
    if (lane < 64) { lchar* z = lds + L_CV + (lane >> 5) * CV_CH + CV_X + (lane & 31) * 16; *(LAS u32x4*)z = (u32x4){0, 0, 0, 0}; }
}
struct ConvAcc { f32x4 acc[4]; };
constexpr int conv_g(int half, int j) { return half ? (j == 0 ? 1 : j == 1 ? 2 : j == 2 ? 5 : 6) : (j == 0 ? 0 : j == 1 ? 3 : j == 2 ? 4 : 7); }
constexpr int conv_slot(int half, int i) { constexpr char a[19] = "323132313231323120", b[19] = "323231323032312021"; return (half ? b[i] : a[i]) - '0'; }
template <int HALF, class HOOK> DI void conv_main_h(lchar* lds, int c2, int lane, ConvAcc& A_, HOOK go) {
    constexpr int NU = HALF ? 7 : 8, NR = 3 * NU;
    lchar* base = lds + L_CV + c2 * CV_CH;
    const int r = lane & 15, q = lane >> 4;
    unsigned apv = (unsigned)(size_t)(base + (r & 3) * CV_CP + ((r >> 2) * 4 - 8 * q + 24) * 2);
    unsigned bpv = (unsigned)(size_t)(base + CV_X + 512 + (16 * r - 16 + 8 * q) * 2);
    asm volatile("" : "+v"(apv), "+v"(bpv));
    f32x4* acc = A_.acc;
#pragma unroll
    for (int g = 0; g < 4; ++g) acc[g] = (f32x4){0, 0, 0, 0};
    bf16x4 Al[2][8], Ah[2][8]; bf16x8 B[2][8];
    auto rd = [&](auto S, int i) {
        constexpr int s = S.value;
        const int u = i / 3, k = i % 3;
        lchar* ap = (lchar*)(size_t)apv; lchar* bp = (lchar*)(size_t)bpv;
        if (k == 0) B[s][u] = lds_b128(bp + 512 * u);
        else if (k == 1) Al[s][u] = *(LAS bf16x4*)(ap + 512 * u);
        else Ah[s][u] = *(LAS bf16x4*)(ap + 512 * u + 8);
    };
    auto step = [&](auto CUR, bool more) {
        constexpr int cur = CUR.value;
        std::integral_constant<int, cur ^ 1> NXT;
        apv += 64; bpv -= 64;
        asm volatile("" : "+v"(apv), "+v"(bpv));
        auto one = [&](auto I) {
            constexpr int i = I.value, sl = conv_slot(HALF, i), g = conv_g(HALF, sl);
            constexpr int u = [] { int n = 0; for (int k = 0; k < i; ++k) n += conv_slot(HALF, k) == sl; return n; }();
            static_assert(u <= g, "order");
            acc[sl] = mfma(cat8(Al[cur][u], Ah[cur][u]), B[cur][g - u], acc[sl]);
            SCHED();
            if (more) { if (i < NR) rd(NXT, i); SCHED(); if (i + 18 < NR) rd(NXT, i + 18); }
            SCHED();
        };
        for_seq(one, std::make_integer_sequence<int, 18>{});
    };
    std::integral_constant<int, 0> I0; std::integral_constant<int, 1> I1;
#pragma unroll
    for (int i = 0; i < NR; ++i) { rd(I0, i); SCHED(); }
    go();
    SCHED();
#pragma unroll 1
    for (int w = 0; w < 3; ++w) { step(I0, true); step(I1, true); }
    step(I0, true); step(I1, false);
}
DI void conv_finish(const Params& p, lchar* lds, int cp, int bw, int lane, unsigned tag, const ConvAcc& A_, unsigned dbits) {
    const int c2 = bw >> 1, half = bw & 1;
    lchar* base = lds + L_CV + c2 * CV_CH;
    const int r = lane & 15, q = lane >> 4;
    const float dch = bf_lo(dbits);
    lchar* stg = lds + L_QY + bw * 4096 + 2048;
#pragma unroll
    for (int j = 0; j < 4; ++j) {
        const f32x4 y = A_.acc[j];
        const int t0 = 256 * (4 * (j >> 1) + (half ? 1 + (j & 1) : 3 * (j & 1))) + 16 * r + 4 * q;
        const u32x2 kv = *(LAS u32x2*)(base + CV_X + 512 + ((t0 & ~7) + 4 - (t0 & 7)) * 2);
        u32x2 o;
        o.x = pk_bf16(y[0] + bf_hi(kv.y) * dch, y[1] + bf_lo(kv.y) * dch);
        o.y = pk_bf16(y[2] + bf_hi(kv.x) * dch, y[3] + bf_lo(kv.x) * dch);
        *(LAS u32x2*)(stg + j * 512 + (16 * r + 4 * q) * 2) = o;
    }
    WAIT_L(0);
    const rsrc_t ry = mk_rsrc(p.ws + WS_Y2T, D_ * T_ * 2);
#pragma unroll
    for (int i = 0; i < 2; ++i) {
        const u32x4 o = *(LAS u32x4*)(stg + i * 1024 + lane * 16);
        const int hi = lane >> 5, g = 4 * i + (half ? 1 + hi : 3 * hi);
        st16_dev(o, ry, (2 * cp + c2) * 4096 + g * 512 + (lane & 31) * 16);
    }
    FENCE();
    if (lane == 0) __hip_atomic_fetch_add((LAS unsigned*)(lds + L_YPUB), 1u, __ATOMIC_RELAXED, __HIP_MEMORY_SCOPE_WORKGROUP);
    publish(p.ws + WS_YFLAG + ((cp >> 5) * 128 + (cp & 31) * 4 + bw) * 4, tag, lane);
}

DI void ldh(bf16x8& d, unsigned voff, rsrc_t r, unsigned soff) {
    asm volatile("buffer_load_dwordx4 %0, %1, %2, %3 offen sc1" : "=v"(d) : "v"(voff), "s"(r), "s"(soff) : "memory");
}
constexpr int g2_nD(int s) { return (s >= 1 && s <= 4) ? 4 : 0; }
constexpr int g2_nH(int s) { return (s >= 0 && s <= 9) ? 4 : 0; }
template <int S> DI constexpr int g2_wait() {
    int n = 0;
    for (int s = S - 3; s <= S - 1; ++s) if (s >= 0) n += g2_nD(s) + g2_nH(s);
    if (S < 3) n += 4 * (3 - S);
    return n;
}
template <int S> DI void g2_step(lchar* lds, int lane, lchar* ring, const int (&ar)[4], const G2W& w, int aw, rsrc_t rh, unsigned hv, unsigned hs, bf16x8 (&H)[5][4], bf16x4 (&Wf)[4][4][2], f32x4 (&acc)[4][4]) {
    constexpr int cur = S & 3, hc = S % 5;
    asm volatile("s_waitcnt vmcnt(%4)" : "+v"(H[hc][0]), "+v"(H[hc][1]), "+v"(H[hc][2]), "+v"(H[hc][3]) : "i"(g2_wait<S>()) : "memory");
    SCHED();
    if (S == LATE_STEP && aw == 0 && lane == 0) *(volatile LAS unsigned*)(lds + L_LATE) = 1;
    const unsigned m0 = w.ldsb + aw * 12288 + ring_off((S + 9) % G2_NS), so = (unsigned)g2_kb(w.rot, aw, S + 9 > 13 ? 13 : S + 9) * 32768;
    const unsigned ho = hs + (unsigned)g2_kb(w.rot, aw, S + 4 > 13 ? 13 : S + 4) * 4096;
#pragma unroll
    for (int n = 0; n < 4; ++n) {
        const bf16x8 wf = cat8(Wf[cur][n][0], Wf[cur][n][1]);
#pragma unroll
        for (int m = 0; m < 4; ++m) {
            acc[n][m] = mfma(wf, H[hc][m], acc[n][m]);
            SCHED();
            const int g = 4 * n + m;
            if (m == 3 && S + 4 <= 13) {
                Wf[cur][n][0] = lds_tr(ring + ring_off((S + 4) % G2_NS) + ar[n]);
                Wf[cur][n][1] = lds_tr(ring + ring_off((S + 4) % G2_NS) + ar[n] + 2048);
            }
            if (g2_nD(S)) {
                if (g == 3) { WAIT_L(2); dma_m0<0>(m0, w.vd[0], w.rw, so); }
                if (g == 5) dma_c<1024>(w.vd[1], w.rw, so);
                if (g == 7) dma_c<2048>(w.vd[2], w.rw, so);
                if (g == 9) dma_c<3072>(w.vd[3], w.rw, so);
                if (g2_nH(S) && g >= 10 && g <= 13) ldh(H[(S + 4) % 5][g - 10], hv, rh, ho + (g - 10) * 1024);
            } else if (g2_nH(S) && (g & 3) == 1) ldh(H[(S + 4) % 5][g >> 2], hv, rh, ho + (g >> 2) * 1024);
            SCHED();
        }
    }
}
DI void gemm2_main(const Params& p, lchar* lds, const G2W& w, int tb, int aw, int lane, unsigned tag) {
    const int c = lane & 15, q = lane >> 4, qq = (lane & 15) >> 2, pp = lane & 3;
    lchar* ring = lds + aw * 12288;
    const int sw = 2 * (q & 1) + (qq >> 1);
    int ar[4];
#pragma unroll
    for (int n = 0; n < 4; ++n) ar[n] = (4 * q + qq) * 128 + ((n ^ sw) << 5) + ((pp >> 1) << 4) + ((pp & 1) << 3);
    f32x4 acc[4][4];
#pragma unroll
    for (int n = 0; n < 4; ++n)
#pragma unroll
        for (int m = 0; m < 4; ++m) acc[n][m] = (f32x4){0, 0, 0, 0};
    bf16x8 H[5][4]; bf16x4 Wf[4][4][2];
    const rsrc_t rh = mk_rsrc(p.ws + WS_H, T_ * F_ * 2);
    const unsigned hv = lane * 16, hs = (unsigned)(tb * 56) * 4096;
    lds_wait(lds + L_RING, 3);
    WAIT_V(8);
#pragma unroll
    for (int s = 0; s < 4; ++s)
#pragma unroll
        for (int g = 0; g < 8; ++g) Wf[s][g >> 1][g & 1] = lds_tr(ring + ring_off(s) + ar[g >> 1] + (g & 1) * 2048);
#pragma unroll
    for (int s = 0; s < 4; ++s)
        asm volatile("s_waitcnt lgkmcnt(0)" : "+v"(Wf[s][0][0]), "+v"(Wf[s][0][1]), "+v"(Wf[s][1][0]), "+v"(Wf[s][1][1]), "+v"(Wf[s][2][0]), "+v"(Wf[s][2][1]), "+v"(Wf[s][3][0]), "+v"(Wf[s][3][1]) :: "memory");
#pragma unroll
    for (int s = 6; s < 10; ++s) g2w_stage(w, aw, ring_off(s % G2_NS), s);
    if (aw == 0) {
        poll<1, 2>(p.ws + WS_HFLAG + (tb >> 1) * 256, 56, tag, lane);
        lds_arrive(lds + L_HRDY, lane);
    } else lds_wait(lds + L_HRDY, 1);
    lds_wait<2>(lds + L_YPUB, 4);
#pragma unroll
    for (int s = 0; s < 4; ++s)
#pragma unroll
        for (int m = 0; m < 4; ++m) ldh(H[s][m], hv, rh, hs + (unsigned)g2_kb(w.rot, aw, s) * 4096 + m * 1024);
    SCHED();
    for_seq([&](auto S) { g2_step<S.value>(lds, lane, ring, ar, w, aw, rh, hv, hs, H, Wf, acc); }, std::make_integer_sequence<int, 14>{});
    WAIT_L(0);
#pragma unroll
    for (int m = 0; m < 4; ++m)
#pragma unroll
        for (int n = 0; n < 4; ++n)
            *(LAS f32x4*)(lds + aw * 12288 + (m < 3 ? m * 4096 : 49152) + c * 256 + (((4 * n + q) ^ c) << 4)) = acc[n][m];
}
struct SideIn { u32x4 qv[2]; };
DI void side_early(const Params& p, int tb, int cg, int bw, int lane, SideIn& in) {
    const rsrc_t rq = mk_rsrc(p.q, T_ * D_ * 2);
#pragma unroll
    for (int i = 0; i < 2; ++i) in.qv[i] = ld16(rq, (64 * tb + 16 * bw + 8 * i + (lane >> 3)) * 1024 + cg * 128 + (lane & 7) * 16);
}
DI void side_late(const Params& p, lchar* lds, int tb, int cg, int bw, int lane, unsigned tag, const SideIn& in) {
    if (bw == 3) {
        lds_wait<8>(lds + L_LATE, 1);
        poll<1, 16>(p.ws + WS_GFLAG + tb * 32, 4, tag, lane);
        const unsigned g = ld4_dev(p.ws + WS_GATE + (64 * tb + lane) * 4);
        *(LAS unsigned*)(lds + L_GATE + lane * 4) = g;
    }
    if (bw == 0) { lds_wait<8>(lds + L_LATE, 1); poll<2, 16>(p.ws + WS_YFLAG + cg * 512, 128, tag, lane); lds_arrive(lds + L_YRDY, lane); }
    else lds_wait<8>(lds + L_YRDY, 1);
    const rsrc_t ry = mk_rsrc(p.ws + WS_Y2T, D_ * T_ * 2);
    u32x4 yv[2];
#pragma unroll
    for (int i = 0; i < 2; ++i) yv[i] = ld16_dev(ry, (64 * cg + 16 * bw + 8 * i + (lane >> 3)) * 4096 + (lane & 7) * 16, tb * 128);
    lchar* slot = lds + L_QY + bw * 4096;
#pragma unroll
    for (int i = 0; i < 2; ++i) {
        const int row = 8 * i + (lane >> 3);
        *(LAS u32x4*)(slot + row * 128 + (((lane & 7) ^ (row & 7)) << 4)) = in.qv[i];
        *(LAS u32x4*)(slot + 2048 + row * 128 + (lane & 7) * 16) = yv[i];
    }
}
DI void final_mix(const Params& p, lchar* lds, int tb, int cg, int wid, int lane) {
    const int c = lane & 15, q = lane >> 4, qq = (lane & 15) >> 2, pp = lane & 3;
    const int tg = wid >> 1, ps = wid & 1;
    const int tok = 16 * tg + c, d0 = 32 * ps + 8 * q;
    const float gate = *(LAS float*)(lds + L_GATE + tok * 4);
    f32x4 b0 = {0, 0, 0, 0}, b1 = {0, 0, 0, 0};
#pragma unroll
    for (int w2 = 0; w2 < 4; ++w2) {
        lchar* pr = lds + w2 * 12288 + (tg < 3 ? tg * 4096 : 49152) + c * 256;
        b0 += *(LAS f32x4*)(pr + ((((d0 >> 2)) ^ c) << 4));
        b1 += *(LAS f32x4*)(pr + ((((d0 >> 2) + 1) ^ c) << 4));
    }
    lchar* ya = lds + L_QY + (d0 >> 4) * 4096 + 2048 + ((d0 & 15) + qq) * 128 + (16 * tg + 4 * pp) * 2;
    const u32x4 yv = __builtin_bit_cast(u32x4, cat8(lds_tr(ya), lds_tr(ya + 512)));
    const u32x4 qv = *(LAS u32x4*)(lds + L_QY + tg * 4096 + c * 128 + (((d0 >> 3) ^ (c & 7)) << 4));
    const float g1 = gate, g0 = 1.0f - gate;
    u32x4 o;
    o.x = pk_bf16(g1 * bf_lo(qv.x) * bf_lo(yv.x) + g0 * b0[0], g1 * bf_hi(qv.x) * bf_hi(yv.x) + g0 * b0[1]);
    o.y = pk_bf16(g1 * bf_lo(qv.y) * bf_lo(yv.y) + g0 * b0[2], g1 * bf_hi(qv.y) * bf_hi(yv.y) + g0 * b0[3]);
    o.z = pk_bf16(g1 * bf_lo(qv.z) * bf_lo(yv.z) + g0 * b1[0], g1 * bf_hi(qv.z) * bf_hi(yv.z) + g0 * b1[1]);
    o.w = pk_bf16(g1 * bf_lo(qv.w) * bf_lo(yv.w) + g0 * b1[2], g1 * bf_hi(qv.w) * bf_hi(yv.w) + g0 * b1[3]);
    lchar* stg = lds + L_OST + wid * 1024;
    *(LAS u32x4*)(stg + c * 64 + ((q ^ ((c >> 1) & 3)) << 4)) = o;
    WAIT_L(0);
    const int r2 = lane >> 2, k2 = lane & 3;
    const u32x4 o2 = *(LAS u32x4*)(stg + r2 * 64 + ((k2 ^ ((r2 >> 1) & 3)) << 4));
    st16_dev(o2, mk_rsrc(p.out, T_ * D_ * 2), (64 * tb + 16 * tg + r2) * 1024 + cg * 128 + ps * 64 + k2 * 16);
}

__global__ void __launch_bounds__(512, 2) gated_mix__ssm_conv_h3__glu_gated_geglu__prompt_kernel(Params p) {
    extern __shared__ __attribute__((aligned(16))) char smem[];
    unsigned long long entry_pc;
    asm volatile("s_getpc_b64 %0" : "=s"(entry_pc));
    lchar* lds = (lchar*)smem;
    const unsigned ldsb = (unsigned)(size_t)lds;
    const int wid = rfl(threadIdx.x >> 6), lane = threadIdx.x & 63;
    const int xcd = blockIdx.x & 7, slot = blockIdx.x >> 3;
    const bool helper = slot >= 28;
    const int fq = xcd >> 1, mh = xcd & 1;
    const int fb = (fq >> 1) * 7 + (fq & 1) * 4;
    const int ft = slot < 24 ? fb + (slot >> 3) : (fq >> 1) * 7 + 3;
    const int mt = slot < 24 ? 8 * mh + (slot & 7) : 8 * mh + 4 * (fq & 1) + (slot - 24);
    const int tb = 16 * mh + 4 * fq + (slot >> 3), cg = slot & 7;
    const int cp = blockIdx.x;
    unsigned tag;
    if (__builtin_expect(wid >= 4, 1)) {
        const int bw = wid - 4;
        u32x4 xv[8]; ConvFix fix;
        const rsrc_t rkv = mk_rsrc(p.ws + WS_KVT + (size_t)cp * 8192, 8192);
        auto load_xv = [&]() {
#pragma unroll
            for (int i = 0; i < 8; ++i) xv[i] = ld16_dev(rkv, lane * 16, i * 1024);
        };
        bool kv_ok = false;
        if (__builtin_expect(!helper, 1)) {
            if (bw < 3) {
                G1Load L; L.init(p, ldsb, mt, ft, bw, lane);
                asm volatile("s_nop 4");
                L.stage(0, 0); L.stage(1, 1);
                int buf = 2;
#pragma unroll 1
                for (int kt = 0; kt < 6; ++kt) {
                    WAIT_V(16); BAR();
                    L.stage(kt + 2, buf);
                    buf = buf == 2 ? 0 : buf + 1;
                }
                WAIT_V(16); BAR();
                WAIT_V(0); BAR();
                tag = rfl(*(LAS unsigned*)(lds + L_TAG));
            } else {
#pragma unroll 1
                for (int kt = 0; kt < 6; ++kt) {
                    BAR();
                    if (kt == SSM_K) touch(ldsb + L_SINK, lane * 128, mk_rsrc(p.ssm_k + (size_t)cp * 8192, 8192));
                    if (kt >= PF_K0) {
                        const int idx = ((kt - PF_K0) * 32 + slot) * 64 + lane, st = idx >> 10, rem = idx & 1023;
                        touch(ldsb + L_SINK, (unsigned)(((14 * (rem >> 8) + st) * 32 + ((rem >> 3) & 31)) * 1024 + (rem & 7) * 128), mk_rsrc(p.wd, F_ * D_ * 2));
                    }
                }
                BAR();
                tag = rfl(*(LAS unsigned*)(lds + L_TAG));
                conv_fix_load(p, cp, lane, fix);
                unsigned f;
                asm volatile("buffer_load_dword %0, %1, %2, 0 offen sc1" : "=v"(f) : "v"((lane & 31) * 4), "s"(mk_rsrc(p.ws + WS_KVFLAG + (cp >> 5) * 128, 128)) : "memory");
                conv_filter_dma(p, ldsb, cp, lane);
                asm volatile("s_waitcnt vmcnt(32)" : "+v"(f) :: "memory");
                kv_ok = __all(f == tag);
                if (kv_ok) load_xv();
                FENCE();
                BAR();
            }
        } else {
            if (wid == 4 && lane > 0 && lane < 16) *(LAS unsigned*)(lds + L_CTL + lane * 4) = 0;
            WAIT_L(0); BAR();
            tag = prep_kv(p, lds, xcd * 4 + (slot - 28), wid, lane, [&]() { if (bw == 3) touch(ldsb + L_SINK, lane * 128, mk_rsrc(p.ssm_k + (size_t)cp * 8192, 8192)); });
            lds_arrive(lds + L_CNTP, lane);
            if (bw == 3) { conv_fix_load(p, cp, lane, fix); conv_filter_dma(p, ldsb, cp, lane); }
        }
        G2W w2; g2w_init(w2, p, ldsb, tb, cg, lane);
        if (bw < 3) {
            if (helper) lds_wait(lds + L_CNTP, 8);
#pragma unroll
            for (int aw = 0; aw < 4; ++aw) g2w_stage(w2, aw, bw * 4096, bw);
        } else {
            if (!kv_ok) { poll<1, 2>(p.ws + WS_KVFLAG + (cp >> 5) * 128, 32, tag, lane); load_xv(); }
            WAIT_V(0);
            conv_fix_write(lds, lane, fix);
#pragma unroll
            for (int i = 0; i < 8; ++i) *(LAS u32x4*)(lds + L_CV + (i >> 2) * CV_CH + CV_X + 512 + (i & 3) * 1024 + lane * 16) = xv[i];
            lds_arrive(lds + L_CVRDY, lane);
        }
        const unsigned dbits = __builtin_amdgcn_raw_buffer_load_b16(mk_rsrc(p.ssm_D, D_ * 2), (2 * cp + (bw >> 1)) * 2, 0, 0);
        SideIn si;
        side_early(p, tb, cg, bw, lane, si);
        if (bw < 3) { WAIT_V(3); lds_arrive(lds + L_RING, lane); }
        lds_wait(lds + L_CVRDY, 1);
        ConvAcc ca;
        auto go = [&]() { lds_wait<2>(lds + L_GELU, 4); };
        if (bw & 1) conv_main_h<1>(lds, bw >> 1, lane, ca, go); else conv_main_h<0>(lds, bw >> 1, lane, ca, go);
        conv_finish(p, lds, cp, bw, lane, tag, ca, dbits);
        side_late(p, lds, tb, cg, bw, lane, tag, si);
        if (blockIdx.x == 0 && bw == 0) {
#pragma unroll 1
            for (int j = 0; j < 8; ++j) poll<2, 16>(p.ws + WS_YFLAG + j * 512, 128, tag, lane);
            if (lane == 0) st4_dev(p.ws + WS_EPOCH, tag);
        }
    } else {
        G2W w;
        if (!helper) {
            if (wid == 0) {
                const unsigned t = mk_tag(ld4_dev(p.ws + WS_EPOCH));
                if (lane < 16) *(LAS unsigned*)(lds + L_CTL + lane * 4) = lane == 0 ? t : 0u;
                WAIT_L(0);
            }
            G1Comp G;
            G.kloop(lds, wid, lane);
            lds_arrive(lds + L_CNTA, lane);
            tag = rfl(*(LAS unsigned*)(lds + L_TAG));
            G.epilogue(p, lds, mt, ft, wid, lane);
            g2w_init(w, p, ldsb, tb, cg, lane);
            publish(p.ws + WS_HFLAG + (mt * 64 + ft * 4 + wid) * 4, tag, lane);
            lds_wait(lds + L_CNTA, 4);
#pragma unroll
            for (int s = 3; s < 6; ++s) g2w_stage(w, wid, ring_off(s), s);
        } else {
            if (slot == 28) {
                touch(ldsb + L_SINK, wid * 8192 + lane * 128, mk_rsrc((const char*)(entry_pc & ~(unsigned long long)255), CODE_PF_KB * 1024));
            }
            unsigned wsw = 0;
            if (slot == 29) {
                const unsigned o = (lane < 8) ? lane * 262144u : 0x7fffffffu;
                if (wid == 0) wsw = __builtin_amdgcn_raw_buffer_load_b32(mk_rsrc(p.ws, WS_END), lane * 262144, 0, 16);
                if (wid == 1) wsw = __builtin_amdgcn_raw_buffer_load_b32(mk_rsrc(p.out, T_ * D_ * 2), o, 0, 16);
                if (wid == 2) touch(ldsb + L_SINK, o, mk_rsrc(p.wd, F_ * D_ * 2));
                if (wid == 3) touch(ldsb + L_SINK, o, mk_rsrc(p.ssm_k, T_ * D_ * 2));
            }
            BAR();
            const int th = xcd * 4 + (slot - 28);
            tag = prep_kv(p, lds, th, wid, lane, []() {});
            asm volatile("" :: "v"(wsw));
            lds_arrive(lds + L_CNTP, lane);
            lds_arrive(lds + L_GELU, lane);
            gate_phase(p, th, wid, lane, tag);
            g2w_init(w, p, ldsb, tb, cg, lane);
            lds_wait(lds + L_CNTP, 8);
#pragma unroll
            for (int s = 3; s < 6; ++s) g2w_stage(w, wid, ring_off(s), s);
        }
        gemm2_main(p, lds, w, tb, wid, lane, tag);
    }
    WAIT_L(0); BAR();
    final_mix(p, lds, tb, cg, wid, lane);
}

extern "C" void kernel_launch(void* const* d_in, const int* in_sizes, int n_in, void* d_out, int out_size, void* d_ws, size_t ws_size, hipStream_t stream) {
    Params p;
    p.q = (const char*)d_in[0]; p.k = (const char*)d_in[1]; p.v = (const char*)d_in[2]; p.shift_k = (const char*)d_in[3];
    p.ssm_k = (const char*)d_in[4]; p.ssm_D = (const char*)d_in[5];
    p.wg = (const char*)d_in[7]; p.wu = (const char*)d_in[8]; p.wd = (const char*)d_in[9]; p.gate_w = (const char*)d_in[10];
    p.out = (char*)d_out;
    p.ws = (char*)d_ws;
    (void)hipFuncSetAttribute((const void*)gated_mix__ssm_conv_h3__glu_gated_geglu__prompt_kernel, hipFuncAttributeMaxDynamicSharedMemorySize, LDS_BYTES);
    gated_mix__ssm_conv_h3__glu_gated_geglu__prompt_kernel<<<256, 512, LDS_BYTES, stream>>>(p);
}
```
